```python
import torch

def _softmax(x, axis=-1):
    return torch.softmax(x, dim=axis)
def _gelu(x):
    return torch.nn.functional.gelu(x, approximate="tanh")
def _silu(x):
    return torch.nn.functional.silu(x)
def _sigmoid(x):
    return torch.sigmoid(x)
def _layernorm(x, axis=-1, eps=1e-5):
    m = x.mean(dim=axis, keepdim=True)
    v = x.var(dim=axis, keepdim=True, unbiased=False)
    return (x - m) / torch.sqrt(v + eps)
def _scatter_add(tgt, idx, src, axis=0):
    return tgt.clone().index_add_(axis, idx, src)
def _segment_sum(src, seg, n):
    out = torch.zeros((n,) + src.shape[1:], dtype=src.dtype)
    return out.index_add_(0, seg, src)
def _pad(x, pw):
    flat = tuple(v for pair in reversed(pw) for v in pair)
    return torch.nn.functional.pad(x, flat)

kernel_name = 'concat_heads__ssm_conv_h3__linear_attn_block__prompt'

def setup_inputs():

    _w = (lambda a: (lambda t: t.to(torch.bfloat16) if t.is_floating_point() else t)(torch.as_tensor(a)))
    def _A_setup():
        B, S, D = 1, 2048, 1024
        ks = 4
        return {
            "q": _w((lambda *s: torch.randn(*s, dtype=torch.bfloat16) * 0.05)(B, S, D)),
            "k": _w((lambda *s: torch.randn(*s, dtype=torch.bfloat16) * 0.4)(B, S, D)),
            "v": _w((lambda *s: torch.randn(*s, dtype=torch.bfloat16) * 0.4)(B, S, D)),
            "shift_k": _w((lambda *s: torch.randn(*s, dtype=torch.bfloat16) * 0.4)(D, ks) * 0.5),
            "ssm_k": _w((lambda *s: torch.randn(*s, dtype=torch.bfloat16) * 1.6)(D, S) * (1.0 / S ** 0.5)),
            "ssm_D": _w((lambda *s: torch.randn(*s, dtype=torch.bfloat16) * 0.4)(D)),
        }
    _A_in = _A_setup()

    def _B_setup():
        B, S, D = 1, 2048, 1024
        return {
            "x": _w((lambda *s: torch.randn(*s, dtype=torch.bfloat16) * 0.05)(B, S, D)),
            "wqkv": _w((lambda *s: torch.randn(*s, dtype=torch.bfloat16) * 0.8)(D, 3 * D)),
            "wo": _w((lambda *s: torch.randn(*s, dtype=torch.bfloat16) * 0.05)(D, D)),
        }
    _B_in = _B_setup()

    return {
        "A_q": _A_in["q"],
        "A_k": _A_in["k"],
        "A_v": _A_in["v"],
        "A_shift_k": _A_in["shift_k"],
        "A_ssm_k": _A_in["ssm_k"],
        "A_ssm_D": _A_in["ssm_D"],
        "B_x": _B_in["x"],
        "B_wqkv": _B_in["wqkv"],
        "B_wo": _B_in["wo"],
    }

def reference(A_q, A_k, A_v, A_shift_k, A_ssm_k, A_ssm_D, B_x, B_wqkv, B_wo):
    def _A_ref(q, k, v, shift_k, ssm_k, ssm_D):
        B, S, D = q.shape
        ks = shift_k.shape[-1]
        k_pad = _pad(k, ((0, 0), (ks - 1, 0), (0, 0)))
        k_shift = torch.zeros_like(k)
        for t in range(ks):
            k_shift = k_shift + k_pad[:, t:t + S, :] * shift_k[:, ks - 1 - t][None, None, :]
        kv = k_shift * v
        L = 2 * S
        kv_pad = _pad(kv, ((0, 0), (0, S), (0, 0)))
        kf_pad = _pad(ssm_k, ((0, 0), (0, S)))
        kv_f = (lambda x, axes=(-2,-1): torch.fft.rfft2(x.float(), dim=axes))(kv_pad[:, None, :, :], axes=(1, 2))
        kf_f = (lambda x, axes=(-2,-1): torch.fft.rfft2(x.float(), dim=axes))(kf_pad[:, None, :], axes=(1, 2))
        kf_f = (lambda x, *d: x.permute(*d))(kf_f, 1, 2, 0)
        y_f = kv_f * kf_f[None, :, :, :]
        y = (lambda x, s, axes=(-2,-1): torch.fft.irfft2(x, s=s, dim=axes).to(torch.bfloat16))(y_f, (1, L), axes=(1, 2))[:, 0, :S, :]
        y = y + kv * ssm_D[None, None, :]
        return q * y

    def _B_ref(x, wqkv, wo):
        B, S, D = x.shape
        H = 32; Dh = D // H
        qkv = (lambda x, *d: x.permute(*d))((x @ wqkv).reshape(B, S, 3, H, Dh), 2, 0, 3, 1, 4)
        q, k, v = qkv[0], qkv[1], qkv[2]
        phi = lambda t: torch.where(t > 0, t + 1, torch.exp(t)) + 1e-6
        pq, pk = phi(q), phi(k)
        kv = torch.einsum("bhsd,bhse->bhde", pk, v)
        z = torch.einsum("bhsd->bhd", pk)
        num = torch.einsum("bhqd,bhde->bhqe", pq, kv)
        den = torch.einsum("bhqd,bhd->bhq", pq, z)[..., None] + 1e-6
        o = num / den
        return (lambda x, *d: x.permute(*d))(o, 0, 2, 1, 3).reshape(B, S, D) @ wo

    a = _A_ref(A_q, A_k, A_v, A_shift_k, A_ssm_k, A_ssm_D)
    b = _B_ref(B_x, B_wqkv, B_wo)
    return torch.concatenate([a, b], axis=-1)
```

```cpp
#define PADAT 0
#include <hip/hip_runtime.h>
#include <stdint.h>

typedef __attribute__((ext_vector_type(8))) __bf16 bf16x8;
typedef __attribute__((ext_vector_type(4))) __bf16 bf16x4;
typedef __attribute__((ext_vector_type(2))) __bf16 bf16x2;
typedef __attribute__((ext_vector_type(4))) float f32x4;
typedef __attribute__((ext_vector_type(2))) float f32x2;
typedef __attribute__((ext_vector_type(4))) unsigned int u32x4;
typedef __attribute__((ext_vector_type(2))) unsigned int u32x2;
typedef unsigned short u16;
#define LDSAS __attribute__((address_space(3)))
#define GAS __attribute__((address_space(1)))
typedef LDSAS char* lptr;
typedef __amdgpu_buffer_rsrc_t rsrc_t;

#define WS_KVT   (0u)
#define WS_Y2T   (8u << 20)
#define WS_KVP   (12u << 20)
#define KVP_SLOT 1152
#define WS_CNT   (13u << 20 | 512u << 10)
#define WS_USED  (14u << 20)
#define WS_STAMP (20u << 20)
#define WS_SPAN (14u << 20)
#define MB_P 0
#define MB_O 1
#define MB_O2 2
#define MB_A 4096
#define MB_Y 4097
#define MB_STRIDE 64
#define MARK 0xFFBFFFBFu

#define G1_STAGE 40960
#define CV_XP 122880
#define CV_X (CV_XP + 896)
#define CV_HP (CV_X + 16384)
#define CV_HS 4128
#define L_SCR 159360
#define L_FLG 163456
#define L_YT 132096
#define F_X1 (L_FLG + 0)
#define F_WC (L_FLG + 144)
#define F_YL (L_FLG + 160)
#define F_T15 (L_FLG + 128)
#define F_X2 (L_FLG + 16)
#define F_HR (L_FLG + 32)
#define F_CVD (L_FLG + 48)
#define F_WH (L_FLG + 64)
#define F_WL (L_FLG + 80)
#define F_NP (L_FLG + 96)
#define F_NO (L_FLG + 100)
#define F_NY (L_FLG + 104)
#define F_PR (L_FLG + 108)
#define F_OR (L_FLG + 112)
#define F_YR (L_FLG + 116)
#define F_AR (L_FLG + 120)
#define SMEM_BYTES 163840
#define X1_Z 156672

#define CODE_BYTES 31500
#define SPIN_LIMIT (1 << 17)

#define ST_DECL
#define ST_OUT
#define STAMP(slot) do { if ((slot) + 100 * ROLE_ == PADAT) tpad_ = __builtin_amdgcn_s_memrealtime(); } while (0)
#define CNT_SLOW(k)
#define PAD_DECL(role) constexpr int ROLE_ = role; unsigned long long tpad_ = 0;
#define PADBLK(b) true
#define PAD_END do { if (tpad_ && PADBLK(blockIdx.x)) { while (__builtin_amdgcn_s_memrealtime() - tpad_ < 4000) __builtin_amdgcn_s_sleep(4); } } while (0)
#define TB_DECL
#define TBAR(t) bar()
#define TB_OUT
struct Params {
  const u16 *q, *k, *v, *shift_k, *ssm_k, *ssm_D, *x, *wqkv, *wo;
  char* ws;
  u16* out;
};

#if defined(__HIP_DEVICE_COMPILE__)
#pragma clang attribute push(__attribute__((target("no-packed-fp32-ops"))), apply_to = function)
#endif
__device__ __forceinline__ float u2f(unsigned x) { return __builtin_bit_cast(float, x); }
__device__ __forceinline__ float i2f(int x) { return __builtin_bit_cast(float, x); }
__device__ __forceinline__ int f2i(float x) { return __builtin_bit_cast(int, x); }
template <int N> struct IC { static constexpr int v = N; };
template <int B, int E, class F> __device__ __forceinline__ void sfor(F f) {
  if constexpr (B < E) { f(IC<B>{}); sfor<B + 1, E>(f); }
}
template <int N> __device__ __forceinline__ void waitv() { asm volatile("s_waitcnt vmcnt(%0)" ::"i"(N) : "memory"); }
__device__ __forceinline__ void waitl0() { asm volatile("s_waitcnt lgkmcnt(0)" ::: "memory"); }
__device__ __forceinline__ void wait_all() { __builtin_amdgcn_s_waitcnt(0x0070); asm volatile("s_waitcnt vmcnt(0) lgkmcnt(0)" ::: "memory"); }
__device__ __forceinline__ void bar() { asm volatile("s_barrier" ::: "memory"); }
__device__ __forceinline__ void sched0() { __builtin_amdgcn_sched_barrier(0); }
__device__ __forceinline__ void cfence() { asm volatile("" ::: "memory"); }

__device__ __forceinline__ rsrc_t mkrs(const void* p, unsigned bytes) {
  return __builtin_amdgcn_make_buffer_rsrc((void*)p, 0, bytes, 0x00020000);
}
__device__ __forceinline__ void dma16(const void* base, lptr dst, unsigned voff, unsigned soff) {
  __builtin_amdgcn_global_load_lds((const GAS void*)((const char*)base + (size_t)soff + voff), (LDSAS void*)dst, 16, 0, 0);
}
__device__ __forceinline__ void dma16s(const void* sbase, unsigned lds_addr, unsigned voff) {
  asm volatile("s_mov_b32 m0, %0\n\ts_nop 0\n\tglobal_load_lds_dwordx4 %1, %2" ::"s"(lds_addr), "v"(voff), "s"(sbase) : "memory");
}
__device__ __forceinline__ void dma4(const void* sbase, unsigned lds_addr, unsigned v0, unsigned v1, unsigned v2, unsigned v3) {
  asm volatile("s_mov_b32 m0, %0\n\ts_nop 0\n\tglobal_load_lds_dwordx4 %2, %1\n\tglobal_load_lds_dwordx4 %3, %1 offset:1024\n\t"
               "global_load_lds_dwordx4 %4, %1 offset:2048\n\tglobal_load_lds_dwordx4 %5, %1 offset:3072"
               ::"s"(lds_addr), "s"(sbase), "v"(v0), "v"(v1), "v"(v2), "v"(v3) : "memory");
}
__device__ __forceinline__ void touch(const void* sbase, unsigned lds_addr, unsigned voff) {
  asm volatile("s_mov_b32 m0, %0\n\ts_nop 0\n\tglobal_load_lds_dword %2, %1" ::"s"(lds_addr), "s"(sbase), "v"(voff) : "memory");
}
__device__ __forceinline__ u32x4 ld_sc1(rsrc_t rs, int voff, int soff) { return __builtin_amdgcn_raw_buffer_load_b128(rs, voff, soff, 16); }
__device__ __forceinline__ void st_sc1(u32x4 v, rsrc_t rs, int voff, int soff) { __builtin_amdgcn_raw_buffer_store_b128(v, rs, voff + soff, 0, 16); }
#define OUTAUX 16
#define OUTAUX3 OUTAUX
template <int AUX> __device__ __forceinline__ void st_out(u32x4 v, void* out, unsigned off) { __builtin_amdgcn_raw_buffer_store_b128(v, mkrs(out, 8u << 20), off, 0, AUX); }
__device__ __forceinline__ float dot2(unsigned a, unsigned b, float c) {
  return __builtin_amdgcn_fdot2_f32_bf16(__builtin_bit_cast(bf16x2, a), __builtin_bit_cast(bf16x2, b), c, false);
}
__device__ __forceinline__ void pin(float& x) { asm volatile("" : "+v"(x)); }
__device__ __forceinline__ unsigned pack2(float lo, float hi) {
  f32x2 v = {lo, hi};
  return __builtin_bit_cast(unsigned, __builtin_convertvector(v, bf16x2));
}
__device__ __forceinline__ float bflo(unsigned w) { return u2f(w << 16); }
__device__ __forceinline__ float bfhi(unsigned w) { return u2f(w & 0xffff0000u); }
__device__ __forceinline__ float phi_f(float t1) {
  const float e = __builtin_amdgcn_exp2f(__builtin_fmaf(t1, 1.44269504088896f, -1.44269504088896f));
  return __builtin_amdgcn_fmed3f(e, t1, 1.f) + 1e-6f;
}
__device__ __forceinline__ bf16x8 trread2(lptr p0, lptr p1) {
  bf16x4 lo = __builtin_amdgcn_ds_read_tr16_b64_v4bf16((LDSAS bf16x4*)p0);
  bf16x4 hi = __builtin_amdgcn_ds_read_tr16_b64_v4bf16((LDSAS bf16x4*)p1);
  return __builtin_shufflevector(lo, hi, 0, 1, 2, 3, 4, 5, 6, 7);
}
#define MFMA16(a, b, c) __builtin_amdgcn_mfma_f32_16x16x32_bf16((a), (b), (c), 0, 0, 0)
#define BC8(x) __builtin_bit_cast(bf16x8, (x))
__device__ __forceinline__ float shr1(float v) {
  return i2f(__builtin_amdgcn_update_dpp(0, f2i(v), 0x138, 0xf, 0xf, true));
}
__device__ __forceinline__ float fsel(bool c, float a, float b) { return c ? a : b; }
__device__ __forceinline__ float ror1(float v) {
  return i2f(__builtin_amdgcn_update_dpp(0, f2i(v), 0x13C, 0xf, 0xf, true));
}
template <int S> __device__ __forceinline__ float row_ror(float v) {
  return i2f(__builtin_amdgcn_update_dpp(0, f2i(v), 0x120 + S, 0xf, 0xf, true));
}

__device__ __forceinline__ void lset(lptr smem, int off, int val) {
  asm volatile("s_mov_b64 exec, 1\n\tds_write_b32 %0, %1\n\ts_mov_b64 exec, -1" ::"v"((unsigned)(size_t)smem + off), "v"(val) : "memory");
}
__device__ __forceinline__ int lmin2(lptr smem, int off) {
  const u32x2 v = *(volatile LDSAS u32x2*)(smem + off);
  return __builtin_amdgcn_readfirstlane((int)(v[0] < v[1] ? v[0] : v[1]));
}
__device__ __forceinline__ int lmin4(lptr smem, int off) {
  const u32x4 v = *(volatile LDSAS u32x4*)(smem + off);
  const unsigned a = v[0] < v[1] ? v[0] : v[1], b = v[2] < v[3] ? v[2] : v[3];
  return __builtin_amdgcn_readfirstlane((int)(a < b ? a : b));
}
__device__ __forceinline__ int lget(lptr smem, int off) {
  return __builtin_amdgcn_readfirstlane(*(volatile LDSAS int*)(smem + off));
}
template <int SLEEP, class F> __device__ __forceinline__ void spin(F ready) {
#pragma unroll 1
  for (int it = 0; it < SPIN_LIMIT; ++it) {
    if (ready()) break;
    if (SLEEP) __builtin_amdgcn_s_sleep(SLEEP);
  }
  cfence();
}
__device__ __forceinline__ GAS unsigned* cnt_ptr(char* ws, int wg) { return (GAS unsigned*)(ws + WS_CNT + wg * MB_STRIDE); }
__device__ __forceinline__ GAS unsigned* ack_ptr(char* ws, int i) { return (GAS unsigned*)(ws + WS_CNT + 32768 + i * 128); }
__device__ __forceinline__ GAS unsigned* hb_ptr(char* ws, int wg) { return (GAS unsigned*)(ws + WS_CNT + 32768 + 1024 + wg * 4); }
__device__ __forceinline__ unsigned gload(GAS unsigned* p) { return __hip_atomic_load(p, __ATOMIC_RELAXED, __HIP_MEMORY_SCOPE_AGENT); }
__device__ __forceinline__ void gpost(char* ws, int kind, int wg, int n, int lane) {
  if (lane < n) __hip_atomic_fetch_add(cnt_ptr(ws, wg) + kind, 1u, __ATOMIC_RELAXED, __HIP_MEMORY_SCOPE_AGENT);
}
__device__ __forceinline__ void gsignal4(char* ws, lptr smem, int cnt, int kind, int wg, int n, int lane) {
  unsigned old;
  asm volatile("s_mov_b64 exec, 1\n\tds_add_rtn_u32 %0, %1, %2\n\ts_mov_b64 exec, -1\n\ts_waitcnt lgkmcnt(0)"
               : "=&v"(old) : "v"((unsigned)(size_t)smem + cnt), "v"(1u) : "memory");
  if (__builtin_amdgcn_readfirstlane(old) == 3) gpost(ws, kind, wg, n, lane);
}
#define PSLEEP 4
__device__ __forceinline__ void gwait(char* ws, int kind, unsigned target) {
  GAS unsigned* p = cnt_ptr(ws, blockIdx.x) + kind;
  spin<PSLEEP>([&]() { return __builtin_amdgcn_readfirstlane(gload(p)) >= target; });
}

#define LDG(dst, voff, base, off) asm volatile("global_load_dwordx4 %0, %1, %2 offset:" #off " sc1" : "=v"(dst) : "v"(voff), "s"(base) : "memory")

struct G1Frag { bf16x8 a[4], b[6]; };

template <int N> __device__ __forceinline__ void post(const char* base, unsigned voff) {
  asm volatile("s_mov_b64 exec, %3\n\tglobal_atomic_add %0, %1, %2\n\ts_mov_b64 exec, -1" ::"v"(voff), "v"(1u), "s"(base), "i"(N == 16 ? 0xffffll : 0xffffffffll) : "memory");
}
__device__ __forceinline__ unsigned arrive(lptr smem, int flag, int cnt) {
  unsigned old;
  asm volatile("s_mov_b64 exec, 1\n\tds_write_b32 %1, %3\n\tds_add_rtn_u32 %0, %2, %3\n\ts_mov_b64 exec, -1"
               : "=&v"(old) : "v"((unsigned)(size_t)smem + flag), "v"((unsigned)(size_t)smem + cnt), "v"(1u) : "memory");
  return old;
}
__device__ __forceinline__ bool was_last(unsigned old) {
  asm volatile("s_waitcnt lgkmcnt(0)" : "+v"(old)::"memory");
  return __builtin_amdgcn_readfirstlane(old) == 3;
}
#define PSL 3
#define POLL1(NEW, OLD) "global_atomic_add " NEW ", v252, v252, %2 sc0\n\ts_waitcnt vmcnt(2)\n\tv_readfirstlane_b32 %1, " OLD "\n\t" \
                        "s_cmp_ge_u32 %1, %3\n\ts_cbranch_scc1 2f\n\ts_sleep %5\n\t"
template <int R> __device__ __forceinline__ void poll_prime(const char* base) {
  if constexpr (R == 0)
    asm volatile("s_mov_b64 exec, 1\n\tv_mov_b32 v252, 0\n\tglobal_atomic_add v253, v252, v252, %0 sc0\n\ts_mov_b64 exec, -1" ::"s"(base) : "memory", "v252", "v253");
  else
    asm volatile("s_mov_b64 exec, 1\n\tv_mov_b32 v252, 0\n\tglobal_atomic_add v254, v252, v252, %0 sc0\n\ts_mov_b64 exec, -1" ::"s"(base) : "memory", "v252", "v254");
}
__device__ __forceinline__ void poll_loop(lptr smem, int flag, const char* base, unsigned target) {
  int it = SPIN_LIMIT;
  unsigned t;
  asm volatile("s_mov_b64 exec, 1\n\tv_mov_b32 v252, 0\n"
               "1:\n\t" POLL1("v255", "v253") POLL1("v253", "v254") POLL1("v254", "v255")
               "s_sub_u32 %0, %0, 1\n\ts_cbranch_scc0 1b\n"
               "2:\n\tv_mov_b32 v252, 1\n\tds_write_b32 %4, v252\n\ts_mov_b64 exec, -1"
               : "+s"(it), "=&s"(t) : "s"(base), "s"(target), "v"((unsigned)(size_t)smem + flag), "i"(PSL)
               : "memory", "scc", "v252", "v253", "v254", "v255");
}
template <int SLEEP, bool PRIME> __device__ __forceinline__ void group_wait(lptr smem, int flag, bool poller, const char* base, unsigned target) {
  if (poller) {
    if (PRIME) { poll_prime<0>(base); __builtin_amdgcn_s_sleep(PSL); poll_prime<1>(base); __builtin_amdgcn_s_sleep(PSL); }
    poll_loop(smem, flag, base, target);
  } else spin<SLEEP>([&]() { return lget(smem, flag) != 0; });
}
#define LDG2(dst, voff, base, off) asm volatile("global_load_dwordx2 %0, %1, %2 offset:" #off " sc1" : "=v"(dst) : "v"(voff), "s"(base) : "memory")
template <int OFF> __device__ __forceinline__ void ldg_sc1(u32x4& dst, unsigned voff, const char* base) {
  asm volatile("global_load_dwordx4 %0, %1, %2 offset:%3 sc1" : "=v"(dst) : "v"(voff), "s"(base), "i"(OFF) : "memory");
}
__device__ __forceinline__ bool any_lane(bool c) { return __builtin_amdgcn_ballot_w64(c) != 0; }
__device__ __forceinline__ bool has_mark(u32x4 v) { return (v[0] == MARK) | (v[1] == MARK) | (v[2] == MARK) | (v[3] == MARK); }
__device__ __forceinline__ unsigned wo_voff(int lane) { return (lane >> 3) * 2048 + (((lane & 7) ^ (((lane >> 4) & 3) << 1)) << 4); }
#define HD(h) ((h) < 30 ? ((h) + 10) % 30 : (h))
#define AH(h) ((int)((0x51764320u >> (4 * ((h) >> 2))) & 15u) * 4 + ((h) & 3))
__device__ __forceinline__ void wo_dma(const Params& P, lptr smem, int nt, unsigned voW, int h0, int n) {
  const unsigned st = 8 * 2048 - 1024;
#pragma unroll 1
  for (int h = h0; h < h0 + n; ++h)
    dma4((const char*)P.wo + nt * 128 + AH(h) * 65536, (unsigned)(size_t)smem + (h < 20 ? h + 10 : h - 20) * 4096, voW, voW + st, voW + 2 * st, voW + 3 * st);
}
#define MK_MODE 0
#define MK_DELAY 10
#define ZERO_DELAY 16
#define BEAT_AT 3
#define G2MAP 0
#define NTX 2
#define QSWAP 1
#define OD 16
#define P_POST 1
#define O_POST 2
#define O2_AT (24 - OD / 2 - (OD >= 16 ? 6 : OD / 2 - 1))
#define PQ_PRE 2
#define W2_POLLER 0
#define W3_EARLY 0
#define SNAKE 1
#define W1N 5
#define W1_GATE 1
#define CSL 0
#define A3_PRIO 0
#define CHK_TO 12
#define G2_PRIO 1
#define H_PRIO 2
#define C_PRIO 3

__device__ __forceinline__ void compute_wave(const Params& P, lptr smem, int wid, int lane, int xl, int idx, const void* pc0) {
  const int l15 = lane & 15, g = lane >> 4;
  const int q4 = l15 >> 2, p4 = lane & 3;
  const int hp = (xl & 3) * 4 + (idx & 3), rb = (xl >> 2) * 8 + (idx >> 2);
  const int hq = hp ^ QSWAP;
  const int nt = G2MAP ? ((idx & 15) ^ NTX) : 8 * (xl & 1) + ((idx & 7) ^ NTX);
  const int wr = wid >> 1, wh = wid & 1;
  PAD_DECL(0)
  constexpr int LAST_ = 10;
  ST_DECL
  STAMP(0);
  if (wid == 0) *(LDSAS int*)(smem + L_FLG + lane * 4) = 0;
  if (wid == 1 && lane < 9 && (idx * 9 + lane) * 128 < CODE_BYTES) touch(pc0, (unsigned)(size_t)smem + L_SCR + 512, (idx * 9 + lane) * 128);
  const char* cntb = P.ws + WS_CNT;
  const char* mbx = cntb + blockIdx.x * MB_STRIDE;
  if (blockIdx.x == 0) {
    const rsrc_t rc = mkrs(cntb, 32768 + 1024);
    __builtin_amdgcn_s_sleep(ZERO_DELAY);
#pragma unroll 1
    for (int i = 0; i < 8; ++i) st_sc1(u32x4{0u, 0u, 0u, 0u}, rc, lane * 16, (wid * 8 + i) * 1024);
    if (wid == 3) st_sc1(u32x4{lane == 0 ? 1u : 0u, 0u, 0u, 0u}, rc, lane * 16, 32768);
  }
  u32x4 pqf[4], pqs[4], whv;
  float pq[4][8];
  f32x4 s0, s1;
  bool oready = false;
  int vst = lane * 16 + (rb * 16 + hq) * 16384 + (wr * 4 + wh) * 2048;
  int vst2 = vst + 4096;
  unsigned vpostO = (G2MAP ? (16 * (rb & 1) + (lane & 15)) * 8 + (rb >> 1) : (8 * (rb & 3) + (lane & 7)) * 8 + 2 * (rb >> 2) + ((lane >> 3) & 1)) * MB_STRIDE;
  lptr xp = smem + L_SCR + (wid ^ 2) * 1024 + lane * 16;
  lptr k0 = smem + L_SCR + (wh + 2) * 1024 + lane * 16 + wr * 8, k1 = smem + L_SCR + wh * 1024 + lane * 16 + (wr ^ 1) * 8;
  lptr k0x = (lptr)((unsigned)(size_t)k0 ^ 8u), k1x = (lptr)((unsigned)(size_t)k1 ^ 8u);
  const rsrc_t ro = mkrs(P.ws + WS_KVT, 4u << 20);
  asm volatile("" : "+v"(vst), "+v"(vst2), "+v"(vpostO), "+v"(xp), "+v"(k0), "+v"(k1), "+v"(k0x), "+v"(k1x));
  unsigned voW = wo_voff(lane);
  asm volatile("" : "+v"(voW));
  {
    f32x4 acc[6][4];
#pragma unroll
    for (int i = 0; i < 6; ++i)
#pragma unroll
      for (int j = 0; j < 4; ++j) { const float c0 = i < 4 ? 1.f : 0.f; acc[i][j] = f32x4{c0, c0, c0, c0}; asm volatile("" : "+v"(acc[i][j])); }
    const int abase = (64 * wr + l15) * 128 + ((g ^ (l15 >> 1)) << 4);
    const int bbase = (16384 + (8 * g + q4) * 128 + (((4 * wh + (p4 >> 1)) ^ (((q4 >> 1) | ((g & 1) << 1)) << 1)) << 4) + (p4 & 1) * 8) ^ (wr << 5);
    lptr pa0 = smem + abase, pa1 = smem + (abase ^ 64), pb0 = smem + bbase, pb1 = smem + (bbase ^ 32);
    auto ld = [&](G1Frag& f, int ks) {
      lptr pa = ks ? pa1 : pa0;
      lptr q0 = pb0 + ks * 4096, q1 = pb1 + ks * 4096;
      f.b[0] = trread2(q0, q0 + 512);
      f.a[0] = *(LDSAS bf16x8*)(pa);
      f.a[1] = *(LDSAS bf16x8*)(pa + 2048);
      f.a[2] = *(LDSAS bf16x8*)(pa + 4096);
      f.a[3] = *(LDSAS bf16x8*)(pa + 6144);
      f.b[1] = trread2(q1, q1 + 512);
      f.b[2] = trread2(q0 + 8192, q0 + 8192 + 512);
      f.b[3] = trread2(q1 + 8192, q1 + 8192 + 512);
      f.b[4] = trread2(q0 + 16384, q0 + 16384 + 512);
      f.b[5] = trread2(q1 + 16384, q1 + 16384 + 512);
    };
    auto mm1 = [&](const G1Frag& f, int i, int j) {
      acc[i][j] = i < 2 ? MFMA16(f.b[i], f.a[j], acc[i][j]) : MFMA16(f.a[j], f.b[i], acc[i][j]);
    };
    auto mm = [&](const G1Frag& f, int i0, int i1) {
#pragma unroll
      for (int i = i0; i < i1; ++i)
#pragma unroll
        for (int j = 0; j < 4; ++j) mm1(f, i, SNAKE && (i & 1) ? 3 - j : j);
    };
    auto ilv = [&]() {
#pragma unroll
      for (int i = 0; i < 16; ++i) {
        __builtin_amdgcn_sched_group_barrier(0x008, 1, 0);
        __builtin_amdgcn_sched_group_barrier(0x100, 1, 0);
      }
      sched0();
    };
    G1Frag F0, F1;
    u32x4 ones = {0x3f803f80u, 0x3f803f80u, 0x3f803f80u, 0x3f803f80u};
    lptr x1w = smem + L_SCR + wid * 1024 + lane * 16, x1r = smem + L_SCR + (wid ^ 2) * 1024 + lane * 16, x1zr = smem + X1_Z + (wid ^ 2) * 64 + g * 16;
    lptr x1zw = smem + X1_Z + wid * 64 + g * 16;
    const rsrc_t rp = mkrs(P.ws + WS_KVP, 1024 * KVP_SLOT);
    int vkvp = lane * 16 + (((hp * 2 + wh) * 2 + wr) * 16 + rb) * KVP_SLOT;
    int vzp = g * 16 + (l15 & 1) * 64 + 1024 + (((hp * 2 + wh) * 2 + wr) * 16 + rb) * KVP_SLOT;
    unsigned vpostP = (((lane & 7) * 4 + (hq & 3)) * 8 + ((lane >> 3) & 1) * 4 + (hq >> 2)) * MB_STRIDE;
    asm volatile("" : "+v"(ones), "+v"(x1w), "+v"(x1r), "+v"(x1zr), "+v"(x1zw), "+v"(vkvp), "+v"(vzp), "+v"(vpostP));
    __builtin_amdgcn_s_waitcnt(0xC07F);
    sched0();
    bar();
    STAMP(1);
    __builtin_amdgcn_s_waitcnt(0xC07F);
    ld(F0, 0);
    sched0();
    int ph = 0, delta = G1_STAGE;
    int beat_at = blockIdx.x == 0 ? (wid == 3 ? BEAT_AT : BEAT_AT - 1) : -1;
    asm volatile("" : "+s"(beat_at));
#pragma unroll 1
    for (int t = 0; t < 15; ++t) {
      ld(F1, 1); mm(F0, 0, 4); ilv();
      mm1(F0, 4, 0); sched0();
      mm1(F0, 4, 1); sched0();
      pa0 += delta; pb0 += delta; asm volatile("" : "+v"(pa0), "+v"(pb0)); sched0();
      mm1(F0, 4, 2); sched0();
      pa1 += delta; pb1 += delta; asm volatile("" : "+v"(pa1), "+v"(pb1)); sched0();
      mm1(F0, 4, 3); mm1(F0, 5, SNAKE ? 3 : 0); mm1(F0, 5, SNAKE ? 2 : 1); mm1(F0, 5, SNAKE ? 1 : 2);
      sched0();
      __builtin_amdgcn_s_waitcnt(0xC07F);
      mm1(F0, 5, SNAKE ? 0 : 3);
      sched0();
      bar();
      sched0();
      ld(F0, 0); mm(F1, 0, 4); ilv();
      mm1(F1, 4, 0); sched0();
      ph = ph == 2 ? 0 : ph + 1;
      asm volatile("" : "+s"(ph)); sched0();
      mm1(F1, 4, 1); sched0();
      delta = ph == 2 ? -2 * G1_STAGE : G1_STAGE;
      asm volatile("" : "+s"(delta)); sched0();
      mm1(F1, 4, 2); sched0();
      if (__builtin_expect(t == beat_at, 0)) {
        waitv<0>();
#pragma unroll 1
        for (int i = 0; i < (wid == 3 ? 4 : 0); ++i) __hip_atomic_fetch_add(hb_ptr(P.ws, i * 64 + lane), 1u, __ATOMIC_RELAXED, __HIP_MEMORY_SCOPE_AGENT);
      }
      sched0();
      mm1(F1, 4, 3);
      mm(F1, 5, 6);
      sched0();
    }
    __builtin_amdgcn_s_setprio(C_PRIO);
    {
      lptr q0 = pb0 + 4096, q1 = pb1 + 4096;
      F1.b[2] = trread2(q0 + 8192, q0 + 8192 + 512);
      F1.a[0] = *(LDSAS bf16x8*)(pa1);
      F1.a[1] = *(LDSAS bf16x8*)(pa1 + 2048);
      F1.a[2] = *(LDSAS bf16x8*)(pa1 + 4096);
      F1.a[3] = *(LDSAS bf16x8*)(pa1 + 6144);
      F1.b[3] = trread2(q1 + 8192, q1 + 8192 + 512);
      mm(F0, 2, 4);
#pragma unroll
      for (int i = 0; i < 8; ++i) {
        __builtin_amdgcn_sched_group_barrier(0x008, 1, 0);
        __builtin_amdgcn_sched_group_barrier(0x100, 1, 0);
      }
      sched0();
      F1.b[4] = trread2(q0 + 16384, q0 + 16384 + 512);
      F1.b[5] = trread2(q1 + 16384, q1 + 16384 + 512);
      F1.b[0] = trread2(q0, q0 + 512);
      F1.b[1] = trread2(q1, q1 + 512);
      mm(F1, 2, 4);
#pragma unroll
      for (int i = 0; i < 8; ++i) {
        __builtin_amdgcn_sched_group_barrier(0x008, 1, 0);
        __builtin_amdgcn_sched_group_barrier(0x100, 1, 0);
      }
      sched0();
      lset(smem, F_T15 + wid * 4, 1);
      sched0();
    }
    STAMP(2);
    u32x4 pkf[2][2], vf[2][2];
    f32x4 kvp[2][2], zz[2];
    const f32x4 zero4 = {0.f, 0.f, 0.f, 0.f};
    auto phik = [&](int u) {
#pragma unroll
      for (int c = 0; c < 2; ++c)
#pragma unroll
        for (int j = 0; j < 4; ++j) {
          const f32x4 kk = acc[2 + c][2 * u + (j >> 1)];
          pkf[u][c][j] = pack2(phi_f(kk[2 * (j & 1)]), phi_f(kk[2 * (j & 1) + 1]));
        }
    };
    auto packv = [&](int u) {
#pragma unroll
      for (int c = 0; c < 2; ++c)
#pragma unroll
        for (int j = 0; j < 4; ++j) {
          const f32x4 vv = acc[4 + c][2 * u + (j >> 1)];
          vf[u][c][j] = pack2(vv[2 * (j & 1)], vv[2 * (j & 1) + 1]);
        }
    };
    mm(F0, 4, 6);
    mm(F1, 4, 6);
    phik(0);
#pragma unroll
    for (int i = 0; i < 16; ++i) {
      __builtin_amdgcn_sched_group_barrier(0x008, 1, 0);
      __builtin_amdgcn_sched_group_barrier(0x402, 4, 0);
    }
    sched0();
    packv(0);
    sched0();
#pragma unroll
    for (int c = 0; c < 2; ++c) {
      kvp[c][1] = MFMA16(BC8(pkf[0][c]), BC8(vf[0][1]), zero4);
      kvp[c][0] = MFMA16(BC8(pkf[0][c]), BC8(vf[0][0]), zero4);
      zz[c] = MFMA16(BC8(pkf[0][c]), BC8(ones), zero4);
    }
    phik(1);
    packv(1);
#pragma unroll
    for (int i = 0; i < 6; ++i) {
      __builtin_amdgcn_sched_group_barrier(0x008, 1, 0);
      __builtin_amdgcn_sched_group_barrier(0x402, 5, 0);
    }
    sched0();
    kvp[0][1] = MFMA16(BC8(pkf[1][0]), BC8(vf[1][1]), kvp[0][1]);
    kvp[1][1] = MFMA16(BC8(pkf[1][1]), BC8(vf[1][1]), kvp[1][1]);
    zz[1] = MFMA16(BC8(pkf[1][1]), BC8(ones), zz[1]);
    sched0();
    kvp[0][0] = MFMA16(BC8(pkf[1][0]), BC8(vf[1][0]), kvp[0][0]);
    kvp[1][0] = MFMA16(BC8(pkf[1][1]), BC8(vf[1][0]), kvp[1][0]);
    zz[0] = MFMA16(BC8(pkf[1][0]), BC8(ones), zz[0]);
    sched0();
    {
      *(LDSAS u32x4*)(x1w) = u32x4{pack2(kvp[1][1][0], kvp[1][1][1]), pack2(kvp[1][1][2], kvp[1][1][3]), pack2(kvp[0][1][0], kvp[0][1][1]), pack2(kvp[0][1][2], kvp[0][1][3])};
      if (l15 == 0) *(LDSAS f32x4*)(x1zw) = zz[1];
      lset(smem, F_X1 + wid * 4, 1);
      u32x4 r;
      f32x4 rz;
      spin<0>([&]() {
        const int f = *(volatile LDSAS int*)(smem + F_X1 + (wid ^ 2) * 4);
        r = *(volatile LDSAS u32x4*)(x1r);
        rz = *(volatile LDSAS f32x4*)(x1zr);
        return __builtin_amdgcn_readfirstlane(f) != 0;
      });
      sched0();
      st_sc1(u32x4{pack2(kvp[0][0][0] + bflo(r[0]), kvp[0][0][1] + bfhi(r[0])), pack2(kvp[0][0][2] + bflo(r[1]), kvp[0][0][3] + bfhi(r[1])),
                   pack2(kvp[1][0][0] + bflo(r[2]), kvp[1][0][1] + bfhi(r[2])), pack2(kvp[1][0][2] + bflo(r[3]), kvp[1][0][3] + bfhi(r[3]))}, rp, vkvp, 0);
      const f32x4 zs = zz[0] + rz;
      asm volatile("s_mov_b64 exec, %3\n\tbuffer_store_dwordx4 %0, %1, %2, 0 offen sc1\n\ts_mov_b64 exec, -1\n\ts_nop 0"
                   ::"v"(zs), "v"(vzp), "s"(rp), "s"(0x0003000300030003ull) : "memory");
      {
        unsigned old;
        asm volatile("s_mov_b64 exec, 1\n\tds_add_rtn_u32 %0, %1, %2\n\ts_mov_b64 exec, -1\n\ts_waitcnt lgkmcnt(0)"
                     : "=&v"(old) : "v"((unsigned)(size_t)smem + F_NP), "v"(1u) : "memory");
        if (__builtin_amdgcn_readfirstlane(old) == 3) post<16>(cntb + MB_P * 4, vpostP);
      }
      sched0();
    }
    STAMP(3);
    auto phiq = [&](int r0, int r1) {
#pragma unroll
      for (int rt = r0; rt < r1; ++rt) {
#pragma unroll
        for (int j = 0; j < 8; ++j) pq[rt][j] = phi_f(acc[j >> 2][rt][j & 3]);
        pqf[rt] = u32x4{pack2(pq[rt][0], pq[rt][1]), pack2(pq[rt][2], pq[rt][3]), pack2(pq[rt][4], pq[rt][5]), pack2(pq[rt][6], pq[rt][7])};
        asm volatile("" : "+v"(pqf[rt]));
        sched0();
      }
    };
    const int headq = hq * 2 + wh;
    const char* zb = P.ws + WS_KVP + headq * 32 * KVP_SLOT + 1024;
    const char* pb = P.ws + WS_KVP + ((headq * 2 + wr) * 16 + 3) * KVP_SLOT;
    const char* pb2 = pb + 6 * KVP_SLOT;
    const char* pb3 = pb + 11 * KVP_SLOT;
    unsigned vz0 = g * 16 + (wr * 16 + l15) * KVP_SLOT, vz1 = g * 16 + ((wr ^ 1) * 16 + l15) * KVP_SLOT, vo = lane * 16;
    static_assert((16 - 2 * W1N) % 3 == 0, "");
    if (W1N < 8 && wid) wo_dma(P, smem, nt, voW, 2 * W1N + (wid - 1) * ((16 - 2 * W1N) / 3), (16 - 2 * W1N) / 3);
    if (wid == 0) poll_prime<0>(mbx + MB_P * 4);
    sched0();
    mm(F0, 0, 2);
    mm(F1, 0, 2);
    sched0();
    phiq(0, PQ_PRE);
    if (wid == 0) poll_prime<1>(mbx + MB_P * 4);
    u32x4 sel0, sel1;
    {
      const unsigned one = g == (l15 >> 2) ? ((l15 & 1) ? 0x3f800000u : 0x00003f80u) : 0u;
      const unsigned e0 = (l15 & 2) ? 0u : one, e1 = (l15 & 2) ? one : 0u;
      sel0 = u32x4{e0, e1, 0u, 0u};
      sel1 = u32x4{0u, 0u, e0, e1};
    }
    asm volatile("" : "+s"(zb), "+s"(pb), "+s"(pb2), "+s"(pb3), "+v"(vz0), "+v"(vz1), "+v"(vo), "+v"(sel0), "+v"(sel1));
    STAMP(4);
    group_wait<CSL, false>(smem, F_PR, wid == 0, mbx + MB_P * 4, 16);
    STAMP(5);
    auto pstage = [&](auto first_) {
      u32x4 z0, z1, pr[16];
      LDG(z0, vz0, zb, 0); LDG(z1, vz1, zb, 0);
      sfor<0, 16>([&](auto p_) {
        constexpr int p = decltype(p_)::v;
        ldg_sc1<(p - (p < 6 ? 3 : p < 12 ? 9 : 14)) * KVP_SLOT>(pr[p], vo, p < 6 ? pb : p < 12 ? pb2 : pb3);
      });
      sched0();
      if constexpr (decltype(first_)::v) phiq(PQ_PRE, 4);
      asm volatile("s_waitcnt vmcnt(16)" : "+v"(z0), "+v"(z1)::"memory");
      float z[8], rd[4];
#pragma unroll
      for (int j = 0; j < 4; ++j) { z[j] = u2f(z0[j]); z[4 + j] = u2f(z1[j]); }
#pragma unroll
      for (int j = 0; j < 8; ++j) z[j] += row_ror<8>(z[j]);
#pragma unroll
      for (int j = 0; j < 8; ++j) z[j] += row_ror<4>(z[j]);
#pragma unroll
      for (int j = 0; j < 8; ++j) z[j] += row_ror<2>(z[j]);
#pragma unroll
      for (int j = 0; j < 8; ++j) z[j] += row_ror<1>(z[j]);
      const u32x4 zf = {pack2(z[0], z[1]), pack2(z[2], z[3]), pack2(z[4], z[5]), pack2(z[6], z[7])};
#pragma unroll
      for (int rt = 0; rt < 4; ++rt) {
        const f32x4 den = MFMA16(BC8(zf), BC8(pqf[rt]), zero4);
        rd[rt] = __builtin_amdgcn_rcpf(den[0] + 1e-6f);
      }
      s0 = zero4; s1 = zero4;
      sfor<0, 4>([&](auto i_) {
        constexpr int i = decltype(i_)::v;
        sched0();
        asm volatile("s_waitcnt vmcnt(%4)" : "+v"(pr[4 * i]), "+v"(pr[4 * i + 1]), "+v"(pr[4 * i + 2]), "+v"(pr[4 * i + 3]) : "i"(12 - 4 * i) : "memory");
        if constexpr (i == 3) { if (wid == 0) poll_prime<0>(mbx + MB_O * 4); }
#pragma unroll
        for (int p = 4 * i; p < 4 * i + 4; ++p) { s0 = MFMA16(BC8(sel0), BC8(pr[p]), s0); s1 = MFMA16(BC8(sel1), BC8(pr[p]), s1); }
        const float r = rd[i];
        pqs[i] = u32x4{pack2(pq[i][0] * r, pq[i][1] * r), pack2(pq[i][2] * r, pq[i][3] * r), pack2(pq[i][4] * r, pq[i][5] * r), pack2(pq[i][6] * r, pq[i][7] * r)};
        asm volatile("" : "+v"(pqs[i]));
#pragma unroll
        for (int j = 0; j < 8; ++j) {
          __builtin_amdgcn_sched_group_barrier(0x008, 1, 0);
          __builtin_amdgcn_sched_group_barrier(0x002, 2, 0);
        }
        sched0();
      });
      whv = *(volatile LDSAS u32x4*)(smem + F_HR);
      if (__builtin_expect(!any_lane(s0[0] != s0[0] || rd[0] != rd[0]), 1)) return true;
      bool m = has_mark(z0) | has_mark(z1);
#pragma unroll
      for (int p = 0; p < 16; ++p) m |= has_mark(pr[p]);
      return !any_lane(m);
    };
    if (__builtin_expect(!pstage(IC<1>{}), 0)) {
#pragma unroll 1
      for (int att = 0; att < SPIN_LIMIT; ++att) {
        if (lane == 0) CNT_SLOW(0);
        if (pstage(IC<0>{})) break;
      }
    }
  }
  {
    const f32x4 zero4 = {0.f, 0.f, 0.f, 0.f};
    STAMP(6);
    *(LDSAS u32x4*)(xp) = u32x4{pack2(s0[0], s0[1]), pack2(s0[2], s0[3]), pack2(s1[0], s1[1]), pack2(s1[2], s1[3])};
    lset(smem, F_X2 + wid * 4, 1);
    if (__builtin_amdgcn_readfirstlane(whv[0] & whv[1] & whv[2] & whv[3]) == 0) spin<0>([&]() { return lmin4(smem, F_HR) != 0; });
    u32x2 k0a, k0b, k1a, k1b;
    spin<0>([&]() {
      const int f = *(volatile LDSAS int*)(smem + F_X2 + (wid ^ 2) * 4);
      k0a = *(volatile LDSAS u32x2*)(k0); k0b = *(volatile LDSAS u32x2*)(k0x); k1a = *(volatile LDSAS u32x2*)(k1); k1b = *(volatile LDSAS u32x2*)(k1x);
      return __builtin_amdgcn_readfirstlane(f) != 0;
    });
    sched0();
    const u32x4 kv0 = {k0a[0], k0a[1], k0b[0], k0b[1]}, kv1 = {k1a[0], k1a[1], k1b[0], k1b[1]};
    f32x4 n0[4], n1[4];
#pragma unroll
    for (int rt = 0; rt < 4; ++rt) {
      n0[rt] = MFMA16(BC8(kv0), BC8(pqs[rt]), zero4);
      n1[rt] = MFMA16(BC8(kv1), BC8(pqs[rt]), zero4);
    }
    sched0();
#pragma unroll
    for (int rt = 0; rt < 4; ++rt) {
      const u32x4 o = {pack2(n0[rt][0], n0[rt][1]), pack2(n0[rt][2], n0[rt][3]), pack2(n1[rt][0], n1[rt][1]), pack2(n1[rt][2], n1[rt][3])};
      st_sc1(o, ro, (rt >> 1) ? vst2 : vst, (rt & 1) * 1024);
      sched0();
    }
    if (wid == 0) { poll_prime<1>(mbx + MB_O * 4); waitv<1>(); } else waitv<0>();
    {
      unsigned old;
      asm volatile("s_mov_b64 exec, 1\n\tds_add_rtn_u32 %0, %1, %2\n\ts_mov_b64 exec, -1\n\ts_waitcnt lgkmcnt(0)"
                   : "=&v"(old) : "v"((unsigned)(size_t)smem + F_NO), "v"(1u) : "memory");
      if (wid == 0) {
        unsigned t;
        asm volatile("v_readfirstlane_b32 %0, v253" : "=s"(t)::"memory");
        oready = __builtin_amdgcn_readfirstlane(t) >= 12u;
        if (oready) lset(smem, F_OR, 1);
      }
      if (__builtin_amdgcn_readfirstlane(old) == 3) post<16>(cntb + ((hq & 6) == 2 ? MB_O2 : MB_O) * 4, vpostO);
    }
    STAMP(7);
  }
  {
    const int rb2 = G2MAP ? 2 * xl + (idx >> 4) : 4 * (xl >> 1) + (idx >> 3);
    const int wb = (4 * g + q4) * 128 + (p4 & 1) * 8 + (((p4 >> 1) ^ ((((g & 1) << 1) | (q4 >> 1)) << 1)) << 4);
    const char* ob = P.ws + WS_KVT + rb2 * 262144 + wid * 4096;
    unsigned vo = lane * 16;
    asm volatile("" : "+v"(vo));
    spin<0>([&]() { return lmin4(smem, F_X2) != 0; });
    if (wid) wo_dma(P, smem, nt, voW, 11 + wid * 5 + W3_EARLY, (wid == 3 ? 4 : 5) - W3_EARLY);
    f32x4 acc[4][2];
    {
      u32x4 of[OD];
      lptr w[4];
#pragma unroll
      for (int T = 0; T < 4; ++T) { w[T] = smem + (wb ^ (T << 5)); asm volatile("" : "+v"(w[T])); }
      bf16x8 a[3][4];
      u32x4 fl;
#pragma unroll
      for (int h = 0; h < 2; ++h)
#pragma unroll
        for (int T = 0; T < 4; ++T) { a[h][T] = trread2(w[T] + HD(h) * 4096, w[T] + HD(h) * 4096 + 2048); sched0(); }
#pragma unroll
      for (int i = 0; i < 4; ++i) { acc[i][0] = f32x4{0.f, 0.f, 0.f, 0.f}; acc[i][1] = acc[i][0]; asm volatile("" : "+v"(acc[i][0]), "+v"(acc[i][1])); }
      if (!oready) group_wait<CSL, false>(smem, F_OR, wid == 0, mbx + MB_O * 4, 12);
      __builtin_amdgcn_s_setprio(G2_PRIO);
      STAMP(8);
      auto ldo = [&](auto f_) {
        constexpr int f = decltype(f_)::v;
        ldg_sc1<(f & 3) * 1024>(of[f % OD], vo, ob + (AH(f >> 1) >> 1) * 16384);
      };
      auto check = [&](auto h_, auto n_) {
        constexpr int h = decltype(h_)::v;
        u32x4 &f0 = of[(2 * h) % OD], &f1 = of[(2 * h + 1) % OD];
        asm volatile("s_waitcnt vmcnt(%2)" : "+v"(f0), "+v"(f1) : "i"(decltype(n_)::v) : "memory");
      };
      sfor<0, OD>(ldo);
      check(IC<0>{}, IC<OD - 2>{});
      sfor<0, 32>([&](auto h_) {
        constexpr int h = decltype(h_)::v;
        constexpr int issued = 2 * h + OD < 64 ? 2 * h + OD : 64;
        __builtin_amdgcn_s_waitcnt(h == 31 ? 0xC07F : 0xC87F);
        sched0();
        if constexpr (h < 30 && (HD(h + 2) >> 4) != (HD(h + 1) >> 4)) {
#pragma unroll
          for (int T = 0; T < 4; ++T) { w[T] += HD(h + 2) >= 16 ? 65536 : -65536; asm volatile("" : "+v"(w[T])); }
        }
        if constexpr (h == 0) { lset(smem, F_WC + wid * 4, 1); sched0(); }
        if constexpr (h == 14 || h == 28) {
          if (__builtin_expect(__builtin_amdgcn_readfirstlane(fl[0] & fl[1] & fl[2] & fl[3]) == 0, 0))
            spin<1>([&]() { return lmin4(smem, h == 14 ? F_WC : F_WL) != 0; });
          sched0();
        }
        if constexpr (h == 13 || h == 27) { fl = *(volatile LDSAS u32x4*)(smem + (h == 13 ? F_WC : F_WL)); sched0(); }
        constexpr int o = (HD(h < 30 ? h + 2 : 0) & 15) * 4096;
        auto half = [&](int T0) {
#pragma unroll
          for (int T = T0; T < T0 + 2; ++T) {
            if constexpr (h < 30) a[(h + 2) % 3][T] = trread2(w[T] + o, w[T] + o + 2048);
            acc[T][0] = MFMA16(a[h % 3][T], BC8(of[(2 * h) % OD]), acc[T][0]);
            acc[T][1] = MFMA16(a[h % 3][T], BC8(of[(2 * h + 1) % OD]), acc[T][1]);
          }
#pragma unroll
          for (int j = 0; j < 4; ++j) {
            __builtin_amdgcn_sched_group_barrier(0x008, 1, 0);
            __builtin_amdgcn_sched_group_barrier(0x100, 1, 0);
          }
          sched0();
        };
        half(0);
        constexpr int LS = 24 - OD / 2;
        static_assert(O2_AT < LS && LS <= O2_AT + OD / 2 - 1, "");
        if constexpr (h < 31) check(IC<h + 1>{}, IC<issued - 2 * h - 4 + (h > O2_AT && h <= LS)>{});
        sched0();
        half(2);
        if constexpr (h == LS) {
          unsigned t;
          asm volatile("s_waitcnt vmcnt(%1)\n\tv_readfirstlane_b32 %0, v253" : "=s"(t) : "i"(2 * (LS - 1 - O2_AT)) : "memory");
          if (__builtin_expect(__builtin_amdgcn_readfirstlane(t) < 4u, 0)) { if (lane == 0) CNT_SLOW(3); group_wait<0, true>(smem, F_OR, true, mbx + MB_O2 * 4, 4); }
          sched0();
        }
        if constexpr (2 * h + OD < 64) { ldo(IC<2 * h + OD>{}); ldo(IC<2 * h + OD + 1>{}); }
        if constexpr (h == O2_AT) poll_prime<0>(mbx + MB_O2 * 4);
        sched0();
      });
    }
    STAMP(9);
#pragma unroll
    for (int st = 0; st < 2; ++st) {
      const unsigned dst = ((rb2 * 128 + wid * 32 + st * 16 + l15) * 2048 + 1024 + nt * 64 + 16 * (g & 1) + 8 * (g >> 1)) * 2;
#pragma unroll
      for (int tp = 0; tp < 2; ++tp) {
        const f32x4 u = acc[2 * tp][st], v = acc[2 * tp + 1][st];
        auto r0 = __builtin_amdgcn_permlane16_swap(pack2(u[0], u[1]), pack2(v[0], v[1]), false, false);
        auto r1 = __builtin_amdgcn_permlane16_swap(pack2(u[2], u[3]), pack2(v[2], v[3]), false, false);
        const u32x4 o = {r0[0], r1[0], r0[1], r1[1]};
        st_out<OUTAUX>(o, P.out, dst + 64 * tp);
      }
    }
  }
  STAMP(10);
  ST_OUT;
  PAD_END;
}

#define CONV_ASM \
  "s_mov_b32 vcc_lo, 0\n\ts_mov_b32 vcc_hi, 0x80000000\n\ts_mov_b64 s[40:41], %14\n\t" \
  "s_mov_b32 s42, %15\n\tds_read_b64_tr_b16 v[34:35], %8 offset:0\n\tds_read_b64_tr_b16 v[36:37], %8 offset:32\n\t" \
  "ds_read_b64_tr_b16 v[98:99], %4 offset:0\n\tds_read_b64_tr_b16 v[100:101], %5 offset:0\n\tds_read_b64_tr_b16 v[102:103], %6 offset:0\n\t" \
  "ds_read_b64_tr_b16 v[104:105], %7 offset:0\n\tds_read_b64_tr_b16 v[38:39], %8 offset:256\n\tds_read_b64_tr_b16 v[40:41], %8 offset:288\n\t" \
  "ds_read_b64_tr_b16 v[106:107], %4 offset:1024\n\tds_read_b64_tr_b16 v[108:109], %5 offset:1024\n\tds_read_b64_tr_b16 v[110:111], %6 offset:1024\n\t" \
  "ds_read_b64_tr_b16 v[112:113], %7 offset:1024\n\tds_read_b64_tr_b16 v[42:43], %8 offset:512\n\tds_read_b64_tr_b16 v[44:45], %8 offset:544\n\t" \
  "ds_read_b64_tr_b16 v[114:115], %4 offset:2048\n\tds_read_b64_tr_b16 v[116:117], %5 offset:2048\n\tds_read_b64_tr_b16 v[118:119], %6 offset:2048\n\t" \
  "ds_read_b64_tr_b16 v[120:121], %7 offset:2048\n\tv_mov_b32 v247, 0\n\tv_mov_b32 v248, 0\n\t" \
  "v_mov_b32 v249, 0\n\ts_waitcnt lgkmcnt(14)\n\tv_mfma_f32_16x16x32_bf16 v[226:229], v[98:101], v[34:37], 0\n\t" \
  "ds_read_b64_tr_b16 v[46:47], %8 offset:768\n\tds_read_b64_tr_b16 v[48:49], %8 offset:800\n\tds_read_b64_tr_b16 v[122:123], %4 offset:3072\n\t" \
  "ds_read_b64_tr_b16 v[124:125], %5 offset:3072\n\tds_read_b64_tr_b16 v[126:127], %6 offset:3072\n\tds_read_b64_tr_b16 v[128:129], %7 offset:3072\n\t" \
  "s_waitcnt lgkmcnt(15)\n\tv_mfma_f32_16x16x32_bf16 v[230:233], v[102:105], v[34:37], 0\n\ts_waitcnt lgkmcnt(14)\n\t" \
  "v_mfma_f32_16x16x32_bf16 v[234:237], v[106:109], v[34:37], 0\n\tds_read_b64_tr_b16 v[50:51], %8 offset:1024\n\tds_read_b64_tr_b16 v[52:53], %8 offset:1056\n\t" \
  "ds_read_b64_tr_b16 v[130:131], %4 offset:4096\n\tds_read_b64_tr_b16 v[132:133], %5 offset:4096\n\tds_read_b64_tr_b16 v[134:135], %6 offset:4096\n\t" \
  "ds_read_b64_tr_b16 v[136:137], %7 offset:4096\n\ts_waitcnt lgkmcnt(15)\n\tv_mfma_f32_16x16x32_bf16 v[238:241], v[110:113], v[34:37], 0\n\t" \
  "v_cndmask_b32_e32 v250, v229, v249, vcc\n\tv_cndmask_b32_e32 v251, v233, v229, vcc\n\ts_nop 0\n\t" \
  "v_add_f32_dpp v228, v250, v228 wave_ror:1 row_mask:0xf bank_mask:0xf bound_ctrl:1\n\tv_add_f32_dpp v232, v251, v232 wave_ror:1 row_mask:0xf bank_mask:0xf bound_ctrl:1\n\tv_mfma_f32_16x16x32_bf16 v[234:237], v[98:101], v[38:41], v[234:237]\n\t" \
  "v_cndmask_b32_e32 v250, v228, v248, vcc\n\tv_cndmask_b32_e32 v251, v232, v228, vcc\n\ts_nop 0\n\t" \
  "v_add_f32_dpp v227, v250, v227 wave_ror:1 row_mask:0xf bank_mask:0xf bound_ctrl:1\n\tv_add_f32_dpp v231, v251, v231 wave_ror:1 row_mask:0xf bank_mask:0xf bound_ctrl:1\n\tv_cndmask_b32_e32 v250, v227, v247, vcc\n\t" \
  "v_cndmask_b32_e32 v251, v231, v227, vcc\n\tv_mfma_f32_16x16x32_bf16 v[238:241], v[102:105], v[38:41], v[238:241]\n\tv_add_f32_dpp v226, v250, v226 wave_ror:1 row_mask:0xf bank_mask:0xf bound_ctrl:1\n\t" \
  "v_add_f32_dpp v230, v251, v230 wave_ror:1 row_mask:0xf bank_mask:0xf bound_ctrl:1\n\tv_cvt_pk_bf16_f32 v252, v226, v230\n\tv_mfma_f32_16x16x32_bf16 v[242:245], v[106:109], v[38:41], 0\n\t" \
  "ds_read_b64_tr_b16 v[54:55], %8 offset:1280\n\tds_read_b64_tr_b16 v[56:57], %8 offset:1312\n\tds_read_b64_tr_b16 v[138:139], %4 offset:5120\n\t" \
  "ds_read_b64_tr_b16 v[140:141], %5 offset:5120\n\tv_mfma_f32_16x16x32_bf16 v[246:249], v[110:113], v[38:41], 0\n\tds_read_b64_tr_b16 v[142:143], %6 offset:5120\n\t" \
  "ds_read_b64_tr_b16 v[144:145], %7 offset:5120\n\tv_cndmask_b32_e32 v250, v237, v233, vcc\n\tv_cndmask_b32_e32 v251, v241, v237, vcc\n\t" \
  "s_waitcnt lgkmcnt(15)\n\tv_mfma_f32_16x16x32_bf16 v[242:245], v[114:117], v[34:37], v[242:245]\n\tv_add_f32_dpp v236, v250, v236 wave_ror:1 row_mask:0xf bank_mask:0xf bound_ctrl:1\n\t" \
  "v_add_f32_dpp v240, v251, v240 wave_ror:1 row_mask:0xf bank_mask:0xf bound_ctrl:1\n\tv_mfma_f32_16x16x32_bf16 v[246:249], v[118:121], v[34:37], v[246:249]\n\tv_cndmask_b32_e32 v250, v236, v232, vcc\n\t" \
  "v_cndmask_b32_e32 v251, v240, v236, vcc\n\ts_nop 0\n\tv_add_f32_dpp v235, v250, v235 wave_ror:1 row_mask:0xf bank_mask:0xf bound_ctrl:1\n\t" \
  "v_add_f32_dpp v239, v251, v239 wave_ror:1 row_mask:0xf bank_mask:0xf bound_ctrl:1\n\tv_mfma_f32_16x16x32_bf16 v[242:245], v[98:101], v[42:45], v[242:245]\n\tv_cndmask_b32_e32 v250, v235, v231, vcc\n\t" \
  "v_cndmask_b32_e32 v251, v239, v235, vcc\n\ts_nop 0\n\tv_add_f32_dpp v234, v250, v234 wave_ror:1 row_mask:0xf bank_mask:0xf bound_ctrl:1\n\t" \
  "v_add_f32_dpp v238, v251, v238 wave_ror:1 row_mask:0xf bank_mask:0xf bound_ctrl:1\n\tv_mfma_f32_16x16x32_bf16 v[246:249], v[102:105], v[42:45], v[246:249]\n\tv_cvt_pk_bf16_f32 v253, v234, v238\n\t" \
  "v_mfma_f32_16x16x32_bf16 v[226:229], v[114:117], v[38:41], 0\n\tds_read_b64_tr_b16 v[58:59], %8 offset:1536\n\tds_read_b64_tr_b16 v[60:61], %8 offset:1568\n\t" \
  "ds_read_b64_tr_b16 v[146:147], %4 offset:6144\n\tds_read_b64_tr_b16 v[148:149], %5 offset:6144\n\tv_mfma_f32_16x16x32_bf16 v[230:233], v[118:121], v[38:41], 0\n\t" \
  "ds_read_b64_tr_b16 v[150:151], %6 offset:6144\n\tds_read_b64_tr_b16 v[152:153], %7 offset:6144\n\tv_mfma_f32_16x16x32_bf16 v[226:229], v[106:109], v[42:45], v[226:229]\n\t" \
  "v_cndmask_b32_e32 v250, v245, v241, vcc\n\tv_cndmask_b32_e32 v251, v249, v245, vcc\n\tv_mfma_f32_16x16x32_bf16 v[230:233], v[110:113], v[42:45], v[230:233]\n\t" \
  "v_add_f32_dpp v244, v250, v244 wave_ror:1 row_mask:0xf bank_mask:0xf bound_ctrl:1\n\tv_add_f32_dpp v248, v251, v248 wave_ror:1 row_mask:0xf bank_mask:0xf bound_ctrl:1\n\ts_waitcnt lgkmcnt(15)\n\t" \
  "v_mfma_f32_16x16x32_bf16 v[226:229], v[122:125], v[34:37], v[226:229]\n\tv_cndmask_b32_e32 v250, v244, v240, vcc\n\tv_cndmask_b32_e32 v251, v248, v244, vcc\n\t" \
  "s_nop 0\n\tv_add_f32_dpp v243, v250, v243 wave_ror:1 row_mask:0xf bank_mask:0xf bound_ctrl:1\n\tv_add_f32_dpp v247, v251, v247 wave_ror:1 row_mask:0xf bank_mask:0xf bound_ctrl:1\n\t" \
  "v_mfma_f32_16x16x32_bf16 v[230:233], v[126:129], v[34:37], v[230:233]\n\tv_cndmask_b32_e32 v250, v243, v239, vcc\n\tv_cndmask_b32_e32 v251, v247, v243, vcc\n\t" \
  "v_mfma_f32_16x16x32_bf16 v[226:229], v[98:101], v[46:49], v[226:229]\n\tv_add_f32_dpp v242, v250, v242 wave_ror:1 row_mask:0xf bank_mask:0xf bound_ctrl:1\n\tv_add_f32_dpp v246, v251, v246 wave_ror:1 row_mask:0xf bank_mask:0xf bound_ctrl:1\n\t" \
  "v_mfma_f32_16x16x32_bf16 v[230:233], v[102:105], v[46:49], v[230:233]\n\tv_cvt_pk_bf16_f32 v254, v242, v246\n\tv_mfma_f32_16x16x32_bf16 v[234:237], v[122:125], v[38:41], 0\n\t" \
  "ds_read_b64_tr_b16 v[62:63], %8 offset:1792\n\tds_read_b64_tr_b16 v[64:65], %8 offset:1824\n\tv_mfma_f32_16x16x32_bf16 v[238:241], v[126:129], v[38:41], 0\n\t" \
  "ds_read_b64_tr_b16 v[154:155], %4 offset:7168\n\tds_read_b64_tr_b16 v[156:157], %5 offset:7168\n\tv_mfma_f32_16x16x32_bf16 v[234:237], v[114:117], v[42:45], v[234:237]\n\t" \
  "ds_read_b64_tr_b16 v[158:159], %6 offset:7168\n\tds_read_b64_tr_b16 v[160:161], %7 offset:7168\n\tv_mfma_f32_16x16x32_bf16 v[238:241], v[118:121], v[42:45], v[238:241]\n\t" \
  "v_cndmask_b32_e32 v250, v229, v249, vcc\n\tv_cndmask_b32_e32 v251, v233, v229, vcc\n\tv_mfma_f32_16x16x32_bf16 v[234:237], v[106:109], v[46:49], v[234:237]\n\t" \
  "v_add_f32_dpp v228, v250, v228 wave_ror:1 row_mask:0xf bank_mask:0xf bound_ctrl:1\n\tv_add_f32_dpp v232, v251, v232 wave_ror:1 row_mask:0xf bank_mask:0xf bound_ctrl:1\n\tv_mfma_f32_16x16x32_bf16 v[238:241], v[110:113], v[46:49], v[238:241]\n\t" \
  "v_cndmask_b32_e32 v250, v228, v248, vcc\n\tv_cndmask_b32_e32 v251, v232, v228, vcc\n\ts_waitcnt lgkmcnt(15)\n\t" \
  "v_mfma_f32_16x16x32_bf16 v[234:237], v[130:133], v[34:37], v[234:237]\n\tv_add_f32_dpp v227, v250, v227 wave_ror:1 row_mask:0xf bank_mask:0xf bound_ctrl:1\n\tv_add_f32_dpp v231, v251, v231 wave_ror:1 row_mask:0xf bank_mask:0xf bound_ctrl:1\n\t" \
  "v_mfma_f32_16x16x32_bf16 v[238:241], v[134:137], v[34:37], v[238:241]\n\tv_cndmask_b32_e32 v250, v227, v247, vcc\n\tv_cndmask_b32_e32 v251, v231, v227, vcc\n\t" \
  "v_mfma_f32_16x16x32_bf16 v[234:237], v[98:101], v[50:53], v[234:237]\n\tv_add_f32_dpp v226, v250, v226 wave_ror:1 row_mask:0xf bank_mask:0xf bound_ctrl:1\n\tv_add_f32_dpp v230, v251, v230 wave_ror:1 row_mask:0xf bank_mask:0xf bound_ctrl:1\n\t" \
  "v_mfma_f32_16x16x32_bf16 v[238:241], v[102:105], v[50:53], v[238:241]\n\tv_cvt_pk_bf16_f32 v255, v226, v230\n\tbuffer_store_dwordx4 v[252:255], %9, %13, 0 offen offset:0 sc1\n\t" \
  "v_mfma_f32_16x16x32_bf16 v[242:245], v[130:133], v[38:41], 0\n\tds_read_b64_tr_b16 v[66:67], %8 offset:2048\n\tds_read_b64_tr_b16 v[68:69], %8 offset:2080\n\t" \
  "v_mfma_f32_16x16x32_bf16 v[246:249], v[134:137], v[38:41], 0\n\tds_read_b64_tr_b16 v[162:163], %4 offset:8192\n\tds_read_b64_tr_b16 v[164:165], %5 offset:8192\n\t" \
  "v_mfma_f32_16x16x32_bf16 v[242:245], v[122:125], v[42:45], v[242:245]\n\tds_read_b64_tr_b16 v[166:167], %6 offset:8192\n\tds_read_b64_tr_b16 v[168:169], %7 offset:8192\n\t" \
  "v_mfma_f32_16x16x32_bf16 v[246:249], v[126:129], v[42:45], v[246:249]\n\tv_cndmask_b32_e32 v250, v237, v233, vcc\n\tv_cndmask_b32_e32 v251, v241, v237, vcc\n\t" \
  "v_mfma_f32_16x16x32_bf16 v[242:245], v[114:117], v[46:49], v[242:245]\n\tv_add_f32_dpp v236, v250, v236 wave_ror:1 row_mask:0xf bank_mask:0xf bound_ctrl:1\n\tv_add_f32_dpp v240, v251, v240 wave_ror:1 row_mask:0xf bank_mask:0xf bound_ctrl:1\n\t" \
  "v_mfma_f32_16x16x32_bf16 v[246:249], v[118:121], v[46:49], v[246:249]\n\tv_cndmask_b32_e32 v250, v236, v232, vcc\n\tv_cndmask_b32_e32 v251, v240, v236, vcc\n\t" \
  "v_mfma_f32_16x16x32_bf16 v[242:245], v[106:109], v[50:53], v[242:245]\n\tv_add_f32_dpp v235, v250, v235 wave_ror:1 row_mask:0xf bank_mask:0xf bound_ctrl:1\n\tv_add_f32_dpp v239, v251, v239 wave_ror:1 row_mask:0xf bank_mask:0xf bound_ctrl:1\n\t" \
  "v_mfma_f32_16x16x32_bf16 v[246:249], v[110:113], v[50:53], v[246:249]\n\ts_waitcnt lgkmcnt(15)\n\tv_mfma_f32_16x16x32_bf16 v[242:245], v[138:141], v[34:37], v[242:245]\n\t" \
  "v_cndmask_b32_e32 v250, v235, v231, vcc\n\tv_cndmask_b32_e32 v251, v239, v235, vcc\n\tv_mfma_f32_16x16x32_bf16 v[246:249], v[142:145], v[34:37], v[246:249]\n\t" \
  "v_add_f32_dpp v234, v250, v234 wave_ror:1 row_mask:0xf bank_mask:0xf bound_ctrl:1\n\tv_add_f32_dpp v238, v251, v238 wave_ror:1 row_mask:0xf bank_mask:0xf bound_ctrl:1\n\tv_mfma_f32_16x16x32_bf16 v[242:245], v[98:101], v[54:57], v[242:245]\n\t" \
  "v_cvt_pk_bf16_f32 v252, v234, v238\n\tv_mfma_f32_16x16x32_bf16 v[246:249], v[102:105], v[54:57], v[246:249]\n\tv_mfma_f32_16x16x32_bf16 v[226:229], v[138:141], v[38:41], 0\n\t" \
  "ds_read_b64_tr_b16 v[70:71], %8 offset:2304\n\tds_read_b64_tr_b16 v[72:73], %8 offset:2336\n\tv_mfma_f32_16x16x32_bf16 v[230:233], v[142:145], v[38:41], 0\n\t" \
  "ds_read_b64_tr_b16 v[170:171], %4 offset:9216\n\tds_read_b64_tr_b16 v[172:173], %5 offset:9216\n\tv_mfma_f32_16x16x32_bf16 v[226:229], v[130:133], v[42:45], v[226:229]\n\t" \
  "ds_read_b64_tr_b16 v[174:175], %6 offset:9216\n\tds_read_b64_tr_b16 v[176:177], %7 offset:9216\n\tv_mfma_f32_16x16x32_bf16 v[230:233], v[134:137], v[42:45], v[230:233]\n\t" \
  "v_cndmask_b32_e32 v250, v245, v241, vcc\n\tv_cndmask_b32_e32 v251, v249, v245, vcc\n\tv_mfma_f32_16x16x32_bf16 v[226:229], v[122:125], v[46:49], v[226:229]\n\t" \
  "v_add_f32_dpp v244, v250, v244 wave_ror:1 row_mask:0xf bank_mask:0xf bound_ctrl:1\n\tv_add_f32_dpp v248, v251, v248 wave_ror:1 row_mask:0xf bank_mask:0xf bound_ctrl:1\n\tv_mfma_f32_16x16x32_bf16 v[230:233], v[126:129], v[46:49], v[230:233]\n\t" \
  "v_mfma_f32_16x16x32_bf16 v[226:229], v[114:117], v[50:53], v[226:229]\n\tv_cndmask_b32_e32 v250, v244, v240, vcc\n\tv_cndmask_b32_e32 v251, v248, v244, vcc\n\t" \
  "v_mfma_f32_16x16x32_bf16 v[230:233], v[118:121], v[50:53], v[230:233]\n\tv_add_f32_dpp v243, v250, v243 wave_ror:1 row_mask:0xf bank_mask:0xf bound_ctrl:1\n\tv_add_f32_dpp v247, v251, v247 wave_ror:1 row_mask:0xf bank_mask:0xf bound_ctrl:1\n\t" \
  "v_mfma_f32_16x16x32_bf16 v[226:229], v[106:109], v[54:57], v[226:229]\n\tv_mfma_f32_16x16x32_bf16 v[230:233], v[110:113], v[54:57], v[230:233]\n\tv_cndmask_b32_e32 v250, v243, v239, vcc\n\t" \
  "v_cndmask_b32_e32 v251, v247, v243, vcc\n\ts_waitcnt lgkmcnt(15)\n\tv_mfma_f32_16x16x32_bf16 v[226:229], v[146:149], v[34:37], v[226:229]\n\t" \
  "v_add_f32_dpp v242, v250, v242 wave_ror:1 row_mask:0xf bank_mask:0xf bound_ctrl:1\n\tv_add_f32_dpp v246, v251, v246 wave_ror:1 row_mask:0xf bank_mask:0xf bound_ctrl:1\n\tv_mfma_f32_16x16x32_bf16 v[230:233], v[150:153], v[34:37], v[230:233]\n\t" \
  "v_mfma_f32_16x16x32_bf16 v[226:229], v[98:101], v[58:61], v[226:229]\n\tv_cvt_pk_bf16_f32 v253, v242, v246\n\tv_mfma_f32_16x16x32_bf16 v[230:233], v[102:105], v[58:61], v[230:233]\n\t" \
  "v_mfma_f32_16x16x32_bf16 v[234:237], v[146:149], v[38:41], 0\n\tds_read_b64_tr_b16 v[74:75], %8 offset:2560\n\tds_read_b64_tr_b16 v[76:77], %8 offset:2592\n\t" \
  "v_mfma_f32_16x16x32_bf16 v[238:241], v[150:153], v[38:41], 0\n\tds_read_b64_tr_b16 v[178:179], %4 offset:10240\n\tds_read_b64_tr_b16 v[180:181], %5 offset:10240\n\t" \
  "v_mfma_f32_16x16x32_bf16 v[234:237], v[138:141], v[42:45], v[234:237]\n\tds_read_b64_tr_b16 v[182:183], %6 offset:10240\n\tds_read_b64_tr_b16 v[184:185], %7 offset:10240\n\t" \
  "v_mfma_f32_16x16x32_bf16 v[238:241], v[142:145], v[42:45], v[238:241]\n\tv_cndmask_b32_e32 v250, v229, v249, vcc\n\tv_cndmask_b32_e32 v251, v233, v229, vcc\n\t" \
  "v_mfma_f32_16x16x32_bf16 v[234:237], v[130:133], v[46:49], v[234:237]\n\tv_add_f32_dpp v228, v250, v228 wave_ror:1 row_mask:0xf bank_mask:0xf bound_ctrl:1\n\tv_add_f32_dpp v232, v251, v232 wave_ror:1 row_mask:0xf bank_mask:0xf bound_ctrl:1\n\t" \
  "v_mfma_f32_16x16x32_bf16 v[238:241], v[134:137], v[46:49], v[238:241]\n\tv_mfma_f32_16x16x32_bf16 v[234:237], v[122:125], v[50:53], v[234:237]\n\tv_cndmask_b32_e32 v250, v228, v248, vcc\n\t" \
  "v_cndmask_b32_e32 v251, v232, v228, vcc\n\tv_mfma_f32_16x16x32_bf16 v[238:241], v[126:129], v[50:53], v[238:241]\n\tv_mfma_f32_16x16x32_bf16 v[234:237], v[114:117], v[54:57], v[234:237]\n\t" \
  "v_add_f32_dpp v227, v250, v227 wave_ror:1 row_mask:0xf bank_mask:0xf bound_ctrl:1\n\tv_add_f32_dpp v231, v251, v231 wave_ror:1 row_mask:0xf bank_mask:0xf bound_ctrl:1\n\tv_mfma_f32_16x16x32_bf16 v[238:241], v[118:121], v[54:57], v[238:241]\n\t" \
  "v_mfma_f32_16x16x32_bf16 v[234:237], v[106:109], v[58:61], v[234:237]\n\tv_cndmask_b32_e32 v250, v227, v247, vcc\n\tv_cndmask_b32_e32 v251, v231, v227, vcc\n\t" \
  "v_mfma_f32_16x16x32_bf16 v[238:241], v[110:113], v[58:61], v[238:241]\n\ts_waitcnt lgkmcnt(15)\n\tv_mfma_f32_16x16x32_bf16 v[234:237], v[154:157], v[34:37], v[234:237]\n\t" \
  "v_add_f32_dpp v226, v250, v226 wave_ror:1 row_mask:0xf bank_mask:0xf bound_ctrl:1\n\tv_add_f32_dpp v230, v251, v230 wave_ror:1 row_mask:0xf bank_mask:0xf bound_ctrl:1\n\tv_mfma_f32_16x16x32_bf16 v[238:241], v[158:161], v[34:37], v[238:241]\n\t" \
  "v_mfma_f32_16x16x32_bf16 v[234:237], v[98:101], v[62:65], v[234:237]\n\tv_cvt_pk_bf16_f32 v254, v226, v230\n\tv_mfma_f32_16x16x32_bf16 v[238:241], v[102:105], v[62:65], v[238:241]\n\t" \
  "v_mfma_f32_16x16x32_bf16 v[242:245], v[154:157], v[38:41], 0\n\tds_read_b64_tr_b16 v[78:79], %8 offset:2816\n\tds_read_b64_tr_b16 v[80:81], %8 offset:2848\n\t" \
  "v_mfma_f32_16x16x32_bf16 v[246:249], v[158:161], v[38:41], 0\n\tds_read_b64_tr_b16 v[186:187], %4 offset:11264\n\tds_read_b64_tr_b16 v[188:189], %5 offset:11264\n\t" \
  "v_mfma_f32_16x16x32_bf16 v[242:245], v[146:149], v[42:45], v[242:245]\n\tds_read_b64_tr_b16 v[190:191], %6 offset:11264\n\tds_read_b64_tr_b16 v[192:193], %7 offset:11264\n\t" \
  "v_mfma_f32_16x16x32_bf16 v[246:249], v[150:153], v[42:45], v[246:249]\n\tv_cndmask_b32_e32 v250, v237, v233, vcc\n\tv_cndmask_b32_e32 v251, v241, v237, vcc\n\t" \
  "v_mfma_f32_16x16x32_bf16 v[242:245], v[138:141], v[46:49], v[242:245]\n\tv_add_f32_dpp v236, v250, v236 wave_ror:1 row_mask:0xf bank_mask:0xf bound_ctrl:1\n\tv_add_f32_dpp v240, v251, v240 wave_ror:1 row_mask:0xf bank_mask:0xf bound_ctrl:1\n\t" \
  "v_mfma_f32_16x16x32_bf16 v[246:249], v[142:145], v[46:49], v[246:249]\n\tv_cndmask_b32_e32 v250, v236, v232, vcc\n\tv_cndmask_b32_e32 v251, v240, v236, vcc\n\t" \
  "v_mfma_f32_16x16x32_bf16 v[242:245], v[130:133], v[50:53], v[242:245]\n\tv_mfma_f32_16x16x32_bf16 v[246:249], v[134:137], v[50:53], v[246:249]\n\tv_add_f32_dpp v235, v250, v235 wave_ror:1 row_mask:0xf bank_mask:0xf bound_ctrl:1\n\t" \
  "v_add_f32_dpp v239, v251, v239 wave_ror:1 row_mask:0xf bank_mask:0xf bound_ctrl:1\n\tv_mfma_f32_16x16x32_bf16 v[242:245], v[122:125], v[54:57], v[242:245]\n\tv_cndmask_b32_e32 v250, v235, v231, vcc\n\t" \
  "v_cndmask_b32_e32 v251, v239, v235, vcc\n\tv_mfma_f32_16x16x32_bf16 v[246:249], v[126:129], v[54:57], v[246:249]\n\tv_add_f32_dpp v234, v250, v234 wave_ror:1 row_mask:0xf bank_mask:0xf bound_ctrl:1\n\t" \
  "v_add_f32_dpp v238, v251, v238 wave_ror:1 row_mask:0xf bank_mask:0xf bound_ctrl:1\n\tv_mfma_f32_16x16x32_bf16 v[242:245], v[114:117], v[58:61], v[242:245]\n\tv_mfma_f32_16x16x32_bf16 v[246:249], v[118:121], v[58:61], v[246:249]\n\t" \
  "v_cvt_pk_bf16_f32 v255, v234, v238\n\tbuffer_store_dwordx4 v[252:255], %9, %13, 0 offen offset:1024 sc1\n\tv_mfma_f32_16x16x32_bf16 v[242:245], v[106:109], v[62:65], v[242:245]\n\t" \
  "s_mov_b64 s[40:41], %16\n\tglobal_load_dwordx4 %0, %11, s[40:41]\n\tv_mfma_f32_16x16x32_bf16 v[246:249], v[110:113], v[62:65], v[246:249]\n\t" \
  "s_add_u32 s40, s40, 0x20000\n\ts_addc_u32 s41, s41, 0\n\tglobal_load_dwordx4 %1, %11, s[40:41]\n\t" \
  "s_waitcnt lgkmcnt(15)\n\tv_mfma_f32_16x16x32_bf16 v[242:245], v[162:165], v[34:37], v[242:245]\n\tv_mfma_f32_16x16x32_bf16 v[246:249], v[166:169], v[34:37], v[246:249]\n\t" \
  "s_add_u32 s40, s40, 0x20000\n\ts_addc_u32 s41, s41, 0\n\tglobal_load_dwordx4 %2, %11, s[40:41]\n\t" \
  "v_mfma_f32_16x16x32_bf16 v[242:245], v[98:101], v[66:69], v[242:245]\n\ts_add_u32 s40, s40, 0x20000\n\ts_addc_u32 s41, s41, 0\n\t" \
  "global_load_dwordx4 %3, %11, s[40:41]\n\tv_mfma_f32_16x16x32_bf16 v[246:249], v[102:105], v[66:69], v[246:249]\n\tv_mfma_f32_16x16x32_bf16 v[226:229], v[162:165], v[38:41], 0\n\t" \
  "ds_read_b64_tr_b16 v[82:83], %8 offset:3072\n\tds_read_b64_tr_b16 v[84:85], %8 offset:3104\n\tv_mfma_f32_16x16x32_bf16 v[230:233], v[166:169], v[38:41], 0\n\t" \
  "ds_read_b64_tr_b16 v[194:195], %4 offset:12288\n\tds_read_b64_tr_b16 v[196:197], %5 offset:12288\n\tv_mfma_f32_16x16x32_bf16 v[226:229], v[154:157], v[42:45], v[226:229]\n\t" \
  "ds_read_b64_tr_b16 v[198:199], %6 offset:12288\n\tds_read_b64_tr_b16 v[200:201], %7 offset:12288\n\tv_mfma_f32_16x16x32_bf16 v[230:233], v[158:161], v[42:45], v[230:233]\n\t" \
  "v_cndmask_b32_e32 v250, v245, v241, vcc\n\tv_cndmask_b32_e32 v251, v249, v245, vcc\n\tv_mfma_f32_16x16x32_bf16 v[226:229], v[146:149], v[46:49], v[226:229]\n\t" \
  "v_mfma_f32_16x16x32_bf16 v[230:233], v[150:153], v[46:49], v[230:233]\n\tv_add_f32_dpp v244, v250, v244 wave_ror:1 row_mask:0xf bank_mask:0xf bound_ctrl:1\n\tv_add_f32_dpp v248, v251, v248 wave_ror:1 row_mask:0xf bank_mask:0xf bound_ctrl:1\n\t" \
  "v_mfma_f32_16x16x32_bf16 v[226:229], v[138:141], v[50:53], v[226:229]\n\tv_mfma_f32_16x16x32_bf16 v[230:233], v[142:145], v[50:53], v[230:233]\n\tv_cndmask_b32_e32 v250, v244, v240, vcc\n\t" \
  "v_cndmask_b32_e32 v251, v248, v244, vcc\n\tv_mfma_f32_16x16x32_bf16 v[226:229], v[130:133], v[54:57], v[226:229]\n\tv_mfma_f32_16x16x32_bf16 v[230:233], v[134:137], v[54:57], v[230:233]\n\t" \
  "v_mfma_f32_16x16x32_bf16 v[226:229], v[122:125], v[58:61], v[226:229]\n\tv_add_f32_dpp v243, v250, v243 wave_ror:1 row_mask:0xf bank_mask:0xf bound_ctrl:1\n\tv_add_f32_dpp v247, v251, v247 wave_ror:1 row_mask:0xf bank_mask:0xf bound_ctrl:1\n\t" \
  "v_mfma_f32_16x16x32_bf16 v[230:233], v[126:129], v[58:61], v[230:233]\n\tv_mfma_f32_16x16x32_bf16 v[226:229], v[114:117], v[62:65], v[226:229]\n\tv_cndmask_b32_e32 v250, v243, v239, vcc\n\t" \
  "v_cndmask_b32_e32 v251, v247, v243, vcc\n\tv_mfma_f32_16x16x32_bf16 v[230:233], v[118:121], v[62:65], v[230:233]\n\tv_mfma_f32_16x16x32_bf16 v[226:229], v[106:109], v[66:69], v[226:229]\n\t" \
  "v_mfma_f32_16x16x32_bf16 v[230:233], v[110:113], v[66:69], v[230:233]\n\tv_add_f32_dpp v242, v250, v242 wave_ror:1 row_mask:0xf bank_mask:0xf bound_ctrl:1\n\tv_add_f32_dpp v246, v251, v246 wave_ror:1 row_mask:0xf bank_mask:0xf bound_ctrl:1\n\t" \
  "s_waitcnt lgkmcnt(15)\n\tv_mfma_f32_16x16x32_bf16 v[226:229], v[170:173], v[34:37], v[226:229]\n\tv_mfma_f32_16x16x32_bf16 v[230:233], v[174:177], v[34:37], v[230:233]\n\t" \
  "v_cvt_pk_bf16_f32 v252, v242, v246\n\tv_mfma_f32_16x16x32_bf16 v[226:229], v[98:101], v[70:73], v[226:229]\n\tv_mfma_f32_16x16x32_bf16 v[230:233], v[102:105], v[70:73], v[230:233]\n\t" \
  "v_mfma_f32_16x16x32_bf16 v[234:237], v[170:173], v[38:41], 0\n\tds_read_b64_tr_b16 v[86:87], %8 offset:3328\n\tds_read_b64_tr_b16 v[88:89], %8 offset:3360\n\t" \
  "v_mfma_f32_16x16x32_bf16 v[238:241], v[174:177], v[38:41], 0\n\tds_read_b64_tr_b16 v[202:203], %4 offset:13312\n\tds_read_b64_tr_b16 v[204:205], %5 offset:13312\n\t" \
  "v_mfma_f32_16x16x32_bf16 v[234:237], v[162:165], v[42:45], v[234:237]\n\tds_read_b64_tr_b16 v[206:207], %6 offset:13312\n\tds_read_b64_tr_b16 v[208:209], %7 offset:13312\n\t" \
  "v_mfma_f32_16x16x32_bf16 v[238:241], v[166:169], v[42:45], v[238:241]\n\tv_cndmask_b32_e32 v250, v229, v249, vcc\n\tv_cndmask_b32_e32 v251, v233, v229, vcc\n\t" \
  "v_mfma_f32_16x16x32_bf16 v[234:237], v[154:157], v[46:49], v[234:237]\n\tv_mfma_f32_16x16x32_bf16 v[238:241], v[158:161], v[46:49], v[238:241]\n\tv_add_f32_dpp v228, v250, v228 wave_ror:1 row_mask:0xf bank_mask:0xf bound_ctrl:1\n\t" \
  "v_add_f32_dpp v232, v251, v232 wave_ror:1 row_mask:0xf bank_mask:0xf bound_ctrl:1\n\tv_mfma_f32_16x16x32_bf16 v[234:237], v[146:149], v[50:53], v[234:237]\n\tv_mfma_f32_16x16x32_bf16 v[238:241], v[150:153], v[50:53], v[238:241]\n\t" \
  "v_mfma_f32_16x16x32_bf16 v[234:237], v[138:141], v[54:57], v[234:237]\n\tv_cndmask_b32_e32 v250, v228, v248, vcc\n\tv_cndmask_b32_e32 v251, v232, v228, vcc\n\t" \
  "v_mfma_f32_16x16x32_bf16 v[238:241], v[142:145], v[54:57], v[238:241]\n\tv_mfma_f32_16x16x32_bf16 v[234:237], v[130:133], v[58:61], v[234:237]\n\tv_mfma_f32_16x16x32_bf16 v[238:241], v[134:137], v[58:61], v[238:241]\n\t" \
  "v_add_f32_dpp v227, v250, v227 wave_ror:1 row_mask:0xf bank_mask:0xf bound_ctrl:1\n\tv_add_f32_dpp v231, v251, v231 wave_ror:1 row_mask:0xf bank_mask:0xf bound_ctrl:1\n\tv_mfma_f32_16x16x32_bf16 v[234:237], v[122:125], v[62:65], v[234:237]\n\t" \
  "v_mfma_f32_16x16x32_bf16 v[238:241], v[126:129], v[62:65], v[238:241]\n\tv_cndmask_b32_e32 v250, v227, v247, vcc\n\tv_cndmask_b32_e32 v251, v231, v227, vcc\n\t" \
  "v_mfma_f32_16x16x32_bf16 v[234:237], v[114:117], v[66:69], v[234:237]\n\tv_mfma_f32_16x16x32_bf16 v[238:241], v[118:121], v[66:69], v[238:241]\n\tv_mfma_f32_16x16x32_bf16 v[234:237], v[106:109], v[70:73], v[234:237]\n\t" \
  "v_add_f32_dpp v226, v250, v226 wave_ror:1 row_mask:0xf bank_mask:0xf bound_ctrl:1\n\tv_add_f32_dpp v230, v251, v230 wave_ror:1 row_mask:0xf bank_mask:0xf bound_ctrl:1\n\tv_mfma_f32_16x16x32_bf16 v[238:241], v[110:113], v[70:73], v[238:241]\n\t" \
  "s_waitcnt lgkmcnt(15)\n\tv_mfma_f32_16x16x32_bf16 v[234:237], v[178:181], v[34:37], v[234:237]\n\tv_mfma_f32_16x16x32_bf16 v[238:241], v[182:185], v[34:37], v[238:241]\n\t" \
  "v_cvt_pk_bf16_f32 v253, v226, v230\n\tv_mfma_f32_16x16x32_bf16 v[234:237], v[98:101], v[74:77], v[234:237]\n\tv_mfma_f32_16x16x32_bf16 v[238:241], v[102:105], v[74:77], v[238:241]\n\t" \
  "v_mfma_f32_16x16x32_bf16 v[242:245], v[178:181], v[38:41], 0\n\tds_read_b64_tr_b16 v[90:91], %8 offset:3584\n\tds_read_b64_tr_b16 v[92:93], %8 offset:3616\n\t" \
  "v_mfma_f32_16x16x32_bf16 v[246:249], v[182:185], v[38:41], 0\n\tds_read_b64_tr_b16 v[210:211], %4 offset:14336\n\tds_read_b64_tr_b16 v[212:213], %5 offset:14336\n\t" \
  "v_mfma_f32_16x16x32_bf16 v[242:245], v[170:173], v[42:45], v[242:245]\n\tds_read_b64_tr_b16 v[214:215], %6 offset:14336\n\tds_read_b64_tr_b16 v[216:217], %7 offset:14336\n\t" \
  "v_mfma_f32_16x16x32_bf16 v[246:249], v[174:177], v[42:45], v[246:249]\n\tv_cndmask_b32_e32 v250, v237, v233, vcc\n\tv_cndmask_b32_e32 v251, v241, v237, vcc\n\t" \
  "v_mfma_f32_16x16x32_bf16 v[242:245], v[162:165], v[46:49], v[242:245]\n\tv_mfma_f32_16x16x32_bf16 v[246:249], v[166:169], v[46:49], v[246:249]\n\tv_mfma_f32_16x16x32_bf16 v[242:245], v[154:157], v[50:53], v[242:245]\n\t" \
  "v_add_f32_dpp v236, v250, v236 wave_ror:1 row_mask:0xf bank_mask:0xf bound_ctrl:1\n\tv_add_f32_dpp v240, v251, v240 wave_ror:1 row_mask:0xf bank_mask:0xf bound_ctrl:1\n\tv_mfma_f32_16x16x32_bf16 v[246:249], v[158:161], v[50:53], v[246:249]\n\t" \
  "v_mfma_f32_16x16x32_bf16 v[242:245], v[146:149], v[54:57], v[242:245]\n\tv_mfma_f32_16x16x32_bf16 v[246:249], v[150:153], v[54:57], v[246:249]\n\tv_cndmask_b32_e32 v250, v236, v232, vcc\n\t" \
  "v_cndmask_b32_e32 v251, v240, v236, vcc\n\tv_mfma_f32_16x16x32_bf16 v[242:245], v[138:141], v[58:61], v[242:245]\n\tv_mfma_f32_16x16x32_bf16 v[246:249], v[142:145], v[58:61], v[246:249]\n\t" \
  "v_mfma_f32_16x16x32_bf16 v[242:245], v[130:133], v[62:65], v[242:245]\n\tv_add_f32_dpp v235, v250, v235 wave_ror:1 row_mask:0xf bank_mask:0xf bound_ctrl:1\n\tv_add_f32_dpp v239, v251, v239 wave_ror:1 row_mask:0xf bank_mask:0xf bound_ctrl:1\n\t" \
  "v_mfma_f32_16x16x32_bf16 v[246:249], v[134:137], v[62:65], v[246:249]\n\tv_mfma_f32_16x16x32_bf16 v[242:245], v[122:125], v[66:69], v[242:245]\n\tv_mfma_f32_16x16x32_bf16 v[246:249], v[126:129], v[66:69], v[246:249]\n\t" \
  "v_cndmask_b32_e32 v250, v235, v231, vcc\n\tv_cndmask_b32_e32 v251, v239, v235, vcc\n\tv_mfma_f32_16x16x32_bf16 v[242:245], v[114:117], v[70:73], v[242:245]\n\t" \
  "v_mfma_f32_16x16x32_bf16 v[246:249], v[118:121], v[70:73], v[246:249]\n\tv_mfma_f32_16x16x32_bf16 v[242:245], v[106:109], v[74:77], v[242:245]\n\tv_add_f32_dpp v234, v250, v234 wave_ror:1 row_mask:0xf bank_mask:0xf bound_ctrl:1\n\t" \
  "v_add_f32_dpp v238, v251, v238 wave_ror:1 row_mask:0xf bank_mask:0xf bound_ctrl:1\n\tv_mfma_f32_16x16x32_bf16 v[246:249], v[110:113], v[74:77], v[246:249]\n\ts_waitcnt lgkmcnt(15)\n\t" \
  "v_mfma_f32_16x16x32_bf16 v[242:245], v[186:189], v[34:37], v[242:245]\n\tv_mfma_f32_16x16x32_bf16 v[246:249], v[190:193], v[34:37], v[246:249]\n\tv_cvt_pk_bf16_f32 v254, v234, v238\n\t" \
  "v_mfma_f32_16x16x32_bf16 v[242:245], v[98:101], v[78:81], v[242:245]\n\tv_mfma_f32_16x16x32_bf16 v[246:249], v[102:105], v[78:81], v[246:249]\n\tv_mfma_f32_16x16x32_bf16 v[226:229], v[186:189], v[38:41], 0\n\t" \
  "ds_read_b64_tr_b16 v[94:95], %8 offset:3840\n\tds_read_b64_tr_b16 v[96:97], %8 offset:3872\n\tv_mfma_f32_16x16x32_bf16 v[230:233], v[190:193], v[38:41], 0\n\t" \
  "ds_read_b64_tr_b16 v[218:219], %4 offset:15360\n\tds_read_b64_tr_b16 v[220:221], %5 offset:15360\n\tv_mfma_f32_16x16x32_bf16 v[226:229], v[178:181], v[42:45], v[226:229]\n\t" \
  "ds_read_b64_tr_b16 v[222:223], %6 offset:15360\n\tds_read_b64_tr_b16 v[224:225], %7 offset:15360\n\tv_mfma_f32_16x16x32_bf16 v[230:233], v[182:185], v[42:45], v[230:233]\n\t" \
  "v_mov_b32 v250, 1\n\ts_mov_b64 exec, 1\n\tds_write_b32 %12, v250 offset:48\n\t" \
  "s_mov_b64 exec, -1\n\tv_mfma_f32_16x16x32_bf16 v[226:229], v[170:173], v[46:49], v[226:229]\n\tv_cndmask_b32_e32 v250, v245, v241, vcc\n\t" \
  "v_cndmask_b32_e32 v251, v249, v245, vcc\n\tv_mfma_f32_16x16x32_bf16 v[230:233], v[174:177], v[46:49], v[230:233]\n\tv_mfma_f32_16x16x32_bf16 v[226:229], v[162:165], v[50:53], v[226:229]\n\t" \
  "v_mfma_f32_16x16x32_bf16 v[230:233], v[166:169], v[50:53], v[230:233]\n\tv_add_f32_dpp v244, v250, v244 wave_ror:1 row_mask:0xf bank_mask:0xf bound_ctrl:1\n\tv_add_f32_dpp v248, v251, v248 wave_ror:1 row_mask:0xf bank_mask:0xf bound_ctrl:1\n\t" \
  "v_mfma_f32_16x16x32_bf16 v[226:229], v[154:157], v[54:57], v[226:229]\n\tv_mfma_f32_16x16x32_bf16 v[230:233], v[158:161], v[54:57], v[230:233]\n\tv_mfma_f32_16x16x32_bf16 v[226:229], v[146:149], v[58:61], v[226:229]\n\t" \
  "v_cndmask_b32_e32 v250, v244, v240, vcc\n\tv_cndmask_b32_e32 v251, v248, v244, vcc\n\tv_mfma_f32_16x16x32_bf16 v[230:233], v[150:153], v[58:61], v[230:233]\n\t" \
  "v_mfma_f32_16x16x32_bf16 v[226:229], v[138:141], v[62:65], v[226:229]\n\tv_mfma_f32_16x16x32_bf16 v[230:233], v[142:145], v[62:65], v[230:233]\n\tv_add_f32_dpp v243, v250, v243 wave_ror:1 row_mask:0xf bank_mask:0xf bound_ctrl:1\n\t" \
  "v_add_f32_dpp v247, v251, v247 wave_ror:1 row_mask:0xf bank_mask:0xf bound_ctrl:1\n\tv_mfma_f32_16x16x32_bf16 v[226:229], v[130:133], v[66:69], v[226:229]\n\tv_mfma_f32_16x16x32_bf16 v[230:233], v[134:137], v[66:69], v[230:233]\n\t" \
  "v_mfma_f32_16x16x32_bf16 v[226:229], v[122:125], v[70:73], v[226:229]\n\tv_cndmask_b32_e32 v250, v243, v239, vcc\n\tv_cndmask_b32_e32 v251, v247, v243, vcc\n\t" \
  "v_mfma_f32_16x16x32_bf16 v[230:233], v[126:129], v[70:73], v[230:233]\n\tv_mfma_f32_16x16x32_bf16 v[226:229], v[114:117], v[74:77], v[226:229]\n\tv_mfma_f32_16x16x32_bf16 v[230:233], v[118:121], v[74:77], v[230:233]\n\t" \
  "v_add_f32_dpp v242, v250, v242 wave_ror:1 row_mask:0xf bank_mask:0xf bound_ctrl:1\n\tv_add_f32_dpp v246, v251, v246 wave_ror:1 row_mask:0xf bank_mask:0xf bound_ctrl:1\n\tv_mfma_f32_16x16x32_bf16 v[226:229], v[106:109], v[78:81], v[226:229]\n\t" \
  "v_mfma_f32_16x16x32_bf16 v[230:233], v[110:113], v[78:81], v[230:233]\n\ts_waitcnt lgkmcnt(15)\n\tv_mfma_f32_16x16x32_bf16 v[226:229], v[194:197], v[34:37], v[226:229]\n\t" \
  "v_cvt_pk_bf16_f32 v255, v242, v246\n\tbuffer_store_dwordx4 v[252:255], %9, %13, 0 offen offset:2048 sc1\n\tv_mfma_f32_16x16x32_bf16 v[230:233], v[198:201], v[34:37], v[230:233]\n\t" \
  "v_mfma_f32_16x16x32_bf16 v[226:229], v[98:101], v[82:85], v[226:229]\n\tv_mfma_f32_16x16x32_bf16 v[230:233], v[102:105], v[82:85], v[230:233]\n\tv_mfma_f32_16x16x32_bf16 v[234:237], v[194:197], v[38:41], 0\n\t" \
  "v_mfma_f32_16x16x32_bf16 v[238:241], v[198:201], v[38:41], 0\n\tv_mfma_f32_16x16x32_bf16 v[234:237], v[186:189], v[42:45], v[234:237]\n\tv_mfma_f32_16x16x32_bf16 v[238:241], v[190:193], v[42:45], v[238:241]\n\t" \
  "s_nop 2\n\tv_cndmask_b32_e32 v250, v229, v249, vcc\n\tv_cndmask_b32_e32 v251, v233, v229, vcc\n\t" \
  "v_mfma_f32_16x16x32_bf16 v[234:237], v[178:181], v[46:49], v[234:237]\n\tv_mfma_f32_16x16x32_bf16 v[238:241], v[182:185], v[46:49], v[238:241]\n\tv_mfma_f32_16x16x32_bf16 v[234:237], v[170:173], v[50:53], v[234:237]\n\t" \
  "v_add_f32_dpp v228, v250, v228 wave_ror:1 row_mask:0xf bank_mask:0xf bound_ctrl:1\n\tv_add_f32_dpp v232, v251, v232 wave_ror:1 row_mask:0xf bank_mask:0xf bound_ctrl:1\n\tv_mfma_f32_16x16x32_bf16 v[238:241], v[174:177], v[50:53], v[238:241]\n\t" \
  "v_mfma_f32_16x16x32_bf16 v[234:237], v[162:165], v[54:57], v[234:237]\n\tv_mfma_f32_16x16x32_bf16 v[238:241], v[166:169], v[54:57], v[238:241]\n\tv_mfma_f32_16x16x32_bf16 v[234:237], v[154:157], v[58:61], v[234:237]\n\t" \
  "v_cndmask_b32_e32 v250, v228, v248, vcc\n\tv_cndmask_b32_e32 v251, v232, v228, vcc\n\tv_mfma_f32_16x16x32_bf16 v[238:241], v[158:161], v[58:61], v[238:241]\n\t" \
  "v_mfma_f32_16x16x32_bf16 v[234:237], v[146:149], v[62:65], v[234:237]\n\tv_mfma_f32_16x16x32_bf16 v[238:241], v[150:153], v[62:65], v[238:241]\n\tv_add_f32_dpp v227, v250, v227 wave_ror:1 row_mask:0xf bank_mask:0xf bound_ctrl:1\n\t" \
  "v_add_f32_dpp v231, v251, v231 wave_ror:1 row_mask:0xf bank_mask:0xf bound_ctrl:1\n\tv_mfma_f32_16x16x32_bf16 v[234:237], v[138:141], v[66:69], v[234:237]\n\tv_mfma_f32_16x16x32_bf16 v[238:241], v[142:145], v[66:69], v[238:241]\n\t" \
  "v_mfma_f32_16x16x32_bf16 v[234:237], v[130:133], v[70:73], v[234:237]\n\tv_mfma_f32_16x16x32_bf16 v[238:241], v[134:137], v[70:73], v[238:241]\n\tv_cndmask_b32_e32 v250, v227, v247, vcc\n\t" \
  "v_cndmask_b32_e32 v251, v231, v227, vcc\n\tv_mfma_f32_16x16x32_bf16 v[234:237], v[122:125], v[74:77], v[234:237]\n\tv_mfma_f32_16x16x32_bf16 v[238:241], v[126:129], v[74:77], v[238:241]\n\t" \
  "v_mfma_f32_16x16x32_bf16 v[234:237], v[114:117], v[78:81], v[234:237]\n\tv_add_f32_dpp v226, v250, v226 wave_ror:1 row_mask:0xf bank_mask:0xf bound_ctrl:1\n\tv_add_f32_dpp v230, v251, v230 wave_ror:1 row_mask:0xf bank_mask:0xf bound_ctrl:1\n\t" \
  "v_mfma_f32_16x16x32_bf16 v[238:241], v[118:121], v[78:81], v[238:241]\n\tv_mfma_f32_16x16x32_bf16 v[234:237], v[106:109], v[82:85], v[234:237]\n\tv_mfma_f32_16x16x32_bf16 v[238:241], v[110:113], v[82:85], v[238:241]\n\t" \
  "v_mfma_f32_16x16x32_bf16 v[234:237], v[202:205], v[34:37], v[234:237]\n\tv_cvt_pk_bf16_f32 v252, v226, v230\n\ts_waitcnt lgkmcnt(13)\n\t" \
  "v_mfma_f32_16x16x32_bf16 v[238:241], v[206:209], v[34:37], v[238:241]\n\tv_mfma_f32_16x16x32_bf16 v[234:237], v[98:101], v[86:89], v[234:237]\n\tv_mfma_f32_16x16x32_bf16 v[238:241], v[102:105], v[86:89], v[238:241]\n\t" \
  "v_mfma_f32_16x16x32_bf16 v[242:245], v[202:205], v[38:41], 0\n\tv_mfma_f32_16x16x32_bf16 v[246:249], v[206:209], v[38:41], 0\n\tv_mfma_f32_16x16x32_bf16 v[242:245], v[194:197], v[42:45], v[242:245]\n\t" \
  "v_mfma_f32_16x16x32_bf16 v[246:249], v[198:201], v[42:45], v[246:249]\n\ts_nop 2\n\tv_cndmask_b32_e32 v250, v237, v233, vcc\n\t" \
  "v_cndmask_b32_e32 v251, v241, v237, vcc\n\tv_mfma_f32_16x16x32_bf16 v[242:245], v[186:189], v[46:49], v[242:245]\n\tv_mfma_f32_16x16x32_bf16 v[246:249], v[190:193], v[46:49], v[246:249]\n\t" \
  "v_mfma_f32_16x16x32_bf16 v[242:245], v[178:181], v[50:53], v[242:245]\n\tv_add_f32_dpp v236, v250, v236 wave_ror:1 row_mask:0xf bank_mask:0xf bound_ctrl:1\n\tv_add_f32_dpp v240, v251, v240 wave_ror:1 row_mask:0xf bank_mask:0xf bound_ctrl:1\n\t" \
  "v_mfma_f32_16x16x32_bf16 v[246:249], v[182:185], v[50:53], v[246:249]\n\tv_mfma_f32_16x16x32_bf16 v[242:245], v[170:173], v[54:57], v[242:245]\n\tv_mfma_f32_16x16x32_bf16 v[246:249], v[174:177], v[54:57], v[246:249]\n\t" \
  "v_mfma_f32_16x16x32_bf16 v[242:245], v[162:165], v[58:61], v[242:245]\n\tv_cndmask_b32_e32 v250, v236, v232, vcc\n\tv_cndmask_b32_e32 v251, v240, v236, vcc\n\t" \
  "v_mfma_f32_16x16x32_bf16 v[246:249], v[166:169], v[58:61], v[246:249]\n\tv_mfma_f32_16x16x32_bf16 v[242:245], v[154:157], v[62:65], v[242:245]\n\tv_mfma_f32_16x16x32_bf16 v[246:249], v[158:161], v[62:65], v[246:249]\n\t" \
  "v_mfma_f32_16x16x32_bf16 v[242:245], v[146:149], v[66:69], v[242:245]\n\tv_add_f32_dpp v235, v250, v235 wave_ror:1 row_mask:0xf bank_mask:0xf bound_ctrl:1\n\tv_add_f32_dpp v239, v251, v239 wave_ror:1 row_mask:0xf bank_mask:0xf bound_ctrl:1\n\t" \
  "v_mfma_f32_16x16x32_bf16 v[246:249], v[150:153], v[66:69], v[246:249]\n\tv_mfma_f32_16x16x32_bf16 v[242:245], v[138:141], v[70:73], v[242:245]\n\tv_mfma_f32_16x16x32_bf16 v[246:249], v[142:145], v[70:73], v[246:249]\n\t" \
  "v_mfma_f32_16x16x32_bf16 v[242:245], v[130:133], v[74:77], v[242:245]\n\tv_cndmask_b32_e32 v250, v235, v231, vcc\n\tv_cndmask_b32_e32 v251, v239, v235, vcc\n\t" \
  "v_mfma_f32_16x16x32_bf16 v[246:249], v[134:137], v[74:77], v[246:249]\n\tv_mfma_f32_16x16x32_bf16 v[242:245], v[122:125], v[78:81], v[242:245]\n\tv_mfma_f32_16x16x32_bf16 v[246:249], v[126:129], v[78:81], v[246:249]\n\t" \
  "v_mfma_f32_16x16x32_bf16 v[242:245], v[114:117], v[82:85], v[242:245]\n\tv_add_f32_dpp v234, v250, v234 wave_ror:1 row_mask:0xf bank_mask:0xf bound_ctrl:1\n\tv_add_f32_dpp v238, v251, v238 wave_ror:1 row_mask:0xf bank_mask:0xf bound_ctrl:1\n\t" \
  "v_mfma_f32_16x16x32_bf16 v[246:249], v[118:121], v[82:85], v[246:249]\n\tv_mfma_f32_16x16x32_bf16 v[242:245], v[106:109], v[86:89], v[242:245]\n\tv_mfma_f32_16x16x32_bf16 v[246:249], v[110:113], v[86:89], v[246:249]\n\t" \
  "s_waitcnt lgkmcnt(9)\n\tv_mfma_f32_16x16x32_bf16 v[242:245], v[210:213], v[34:37], v[242:245]\n\tv_cvt_pk_bf16_f32 v253, v234, v238\n\t" \
  "s_waitcnt lgkmcnt(7)\n\tv_mfma_f32_16x16x32_bf16 v[246:249], v[214:217], v[34:37], v[246:249]\n\tv_mfma_f32_16x16x32_bf16 v[242:245], v[98:101], v[90:93], v[242:245]\n\t" \
  "v_mfma_f32_16x16x32_bf16 v[246:249], v[102:105], v[90:93], v[246:249]\n\tv_mfma_f32_16x16x32_bf16 v[226:229], v[210:213], v[38:41], 0\n\tv_mfma_f32_16x16x32_bf16 v[230:233], v[214:217], v[38:41], 0\n\t" \
  "v_mfma_f32_16x16x32_bf16 v[226:229], v[202:205], v[42:45], v[226:229]\n\tv_mfma_f32_16x16x32_bf16 v[230:233], v[206:209], v[42:45], v[230:233]\n\ts_nop 2\n\t" \
  "v_cndmask_b32_e32 v250, v245, v241, vcc\n\tv_cndmask_b32_e32 v251, v249, v245, vcc\n\tv_mfma_f32_16x16x32_bf16 v[226:229], v[194:197], v[46:49], v[226:229]\n\t" \
  "v_mfma_f32_16x16x32_bf16 v[230:233], v[198:201], v[46:49], v[230:233]\n\tv_mfma_f32_16x16x32_bf16 v[226:229], v[186:189], v[50:53], v[226:229]\n\tv_mfma_f32_16x16x32_bf16 v[230:233], v[190:193], v[50:53], v[230:233]\n\t" \
  "v_add_f32_dpp v244, v250, v244 wave_ror:1 row_mask:0xf bank_mask:0xf bound_ctrl:1\n\tv_add_f32_dpp v248, v251, v248 wave_ror:1 row_mask:0xf bank_mask:0xf bound_ctrl:1\n\tv_mfma_f32_16x16x32_bf16 v[226:229], v[178:181], v[54:57], v[226:229]\n\t" \
  "v_mfma_f32_16x16x32_bf16 v[230:233], v[182:185], v[54:57], v[230:233]\n\tv_mfma_f32_16x16x32_bf16 v[226:229], v[170:173], v[58:61], v[226:229]\n\tv_mfma_f32_16x16x32_bf16 v[230:233], v[174:177], v[58:61], v[230:233]\n\t" \
  "v_cndmask_b32_e32 v250, v244, v240, vcc\n\tv_cndmask_b32_e32 v251, v248, v244, vcc\n\tv_mfma_f32_16x16x32_bf16 v[226:229], v[162:165], v[62:65], v[226:229]\n\t" \
  "v_mfma_f32_16x16x32_bf16 v[230:233], v[166:169], v[62:65], v[230:233]\n\tv_mfma_f32_16x16x32_bf16 v[226:229], v[154:157], v[66:69], v[226:229]\n\tv_mfma_f32_16x16x32_bf16 v[230:233], v[158:161], v[66:69], v[230:233]\n\t" \
  "v_add_f32_dpp v243, v250, v243 wave_ror:1 row_mask:0xf bank_mask:0xf bound_ctrl:1\n\tv_add_f32_dpp v247, v251, v247 wave_ror:1 row_mask:0xf bank_mask:0xf bound_ctrl:1\n\tv_mfma_f32_16x16x32_bf16 v[226:229], v[146:149], v[70:73], v[226:229]\n\t" \
  "v_mfma_f32_16x16x32_bf16 v[230:233], v[150:153], v[70:73], v[230:233]\n\tv_mfma_f32_16x16x32_bf16 v[226:229], v[138:141], v[74:77], v[226:229]\n\tv_mfma_f32_16x16x32_bf16 v[230:233], v[142:145], v[74:77], v[230:233]\n\t" \
  "v_cndmask_b32_e32 v250, v243, v239, vcc\n\tv_cndmask_b32_e32 v251, v247, v243, vcc\n\tv_mfma_f32_16x16x32_bf16 v[226:229], v[130:133], v[78:81], v[226:229]\n\t" \
  "v_mfma_f32_16x16x32_bf16 v[230:233], v[134:137], v[78:81], v[230:233]\n\tv_mfma_f32_16x16x32_bf16 v[226:229], v[122:125], v[82:85], v[226:229]\n\tv_mfma_f32_16x16x32_bf16 v[230:233], v[126:129], v[82:85], v[230:233]\n\t" \
  "v_add_f32_dpp v242, v250, v242 wave_ror:1 row_mask:0xf bank_mask:0xf bound_ctrl:1\n\tv_add_f32_dpp v246, v251, v246 wave_ror:1 row_mask:0xf bank_mask:0xf bound_ctrl:1\n\tv_mfma_f32_16x16x32_bf16 v[226:229], v[114:117], v[86:89], v[226:229]\n\t" \
  "v_mfma_f32_16x16x32_bf16 v[230:233], v[118:121], v[86:89], v[230:233]\n\tv_mfma_f32_16x16x32_bf16 v[226:229], v[106:109], v[90:93], v[226:229]\n\tv_mfma_f32_16x16x32_bf16 v[230:233], v[110:113], v[90:93], v[230:233]\n\t" \
  "v_cvt_pk_bf16_f32 v254, v242, v246\n\ts_waitcnt lgkmcnt(3)\n\tv_mfma_f32_16x16x32_bf16 v[226:229], v[218:221], v[34:37], v[226:229]\n\t" \
  "s_waitcnt lgkmcnt(1)\n\tv_mfma_f32_16x16x32_bf16 v[230:233], v[222:225], v[34:37], v[230:233]\n\tv_mfma_f32_16x16x32_bf16 v[226:229], v[98:101], v[94:97], v[226:229]\n\t" \
  "v_mfma_f32_16x16x32_bf16 v[230:233], v[102:105], v[94:97], v[230:233]\n\ts_nop 7\n\tv_cndmask_b32_e32 v250, v229, v249, vcc\n\t" \
  "v_cndmask_b32_e32 v251, v233, v229, vcc\n\ts_nop 0\n\tv_add_f32_dpp v228, v250, v228 wave_ror:1 row_mask:0xf bank_mask:0xf bound_ctrl:1\n\t" \
  "v_add_f32_dpp v232, v251, v232 wave_ror:1 row_mask:0xf bank_mask:0xf bound_ctrl:1\n\tv_cndmask_b32_e32 v250, v228, v248, vcc\n\tv_cndmask_b32_e32 v251, v232, v228, vcc\n\t" \
  "s_nop 0\n\tv_add_f32_dpp v227, v250, v227 wave_ror:1 row_mask:0xf bank_mask:0xf bound_ctrl:1\n\tv_add_f32_dpp v231, v251, v231 wave_ror:1 row_mask:0xf bank_mask:0xf bound_ctrl:1\n\t" \
  "v_cndmask_b32_e32 v250, v227, v247, vcc\n\tv_cndmask_b32_e32 v251, v231, v227, vcc\n\ts_nop 0\n\t" \
  "v_add_f32_dpp v226, v250, v226 wave_ror:1 row_mask:0xf bank_mask:0xf bound_ctrl:1\n\tv_add_f32_dpp v230, v251, v230 wave_ror:1 row_mask:0xf bank_mask:0xf bound_ctrl:1\n\tv_cvt_pk_bf16_f32 v255, v226, v230\n\t" \
  "buffer_store_dwordx4 v[252:255], %9, %13, 0 offen offset:3072 sc1\n\ts_nop 1\n\t"
#define CONV_CLOBBERS "v34", "v35", "v36", "v37", "v38", "v39", "v40", "v41", "v42", "v43", "v44", "v45", "v46", "v47", "v48", "v49", "v50", "v51", "v52", "v53", "v54", "v55", "v56", "v57", "v58", "v59", "v60", "v61", "v62", "v63", "v64", "v65", "v66", "v67", "v68", "v69", "v70", "v71", "v72", "v73", "v74", "v75", "v76", "v77", "v78", "v79", "v80", "v81", "v82", "v83", "v84", "v85", "v86", "v87", "v88", "v89", "v90", "v91", "v92", "v93", "v94", "v95", "v96", "v97", "v98", "v99", "v100", "v101", "v102", "v103", "v104", "v105", "v106", "v107", "v108", "v109", "v110", "v111", "v112", "v113", "v114", "v115", "v116", "v117", "v118", "v119", "v120", "v121", "v122", "v123", "v124", "v125", "v126", "v127", "v128", "v129", "v130", "v131", "v132", "v133", "v134", "v135", "v136", "v137", "v138", "v139", "v140", "v141", "v142", "v143", "v144", "v145", "v146", "v147", "v148", "v149", "v150", "v151", "v152", "v153", "v154", "v155", "v156", "v157", "v158", "v159", "v160", "v161", "v162", "v163", "v164", "v165", "v166", "v167", "v168", "v169", "v170", "v171", "v172", "v173", "v174", "v175", "v176", "v177", "v178", "v179", "v180", "v181", "v182", "v183", "v184", "v185", "v186", "v187", "v188", "v189", "v190", "v191", "v192", "v193", "v194", "v195", "v196", "v197", "v198", "v199", "v200", "v201", "v202", "v203", "v204", "v205", "v206", "v207", "v208", "v209", "v210", "v211", "v212", "v213", "v214", "v215", "v216", "v217", "v218", "v219", "v220", "v221", "v222", "v223", "v224", "v225", "v226", "v227", "v228", "v229", "v230", "v231", "v232", "v233", "v234", "v235", "v236", "v237", "v238", "v239", "v240", "v241", "v242", "v243", "v244", "v245", "v246", "v247", "v248", "v249", "v250", "v251", "v252", "v253", "v254", "v255"
#define G1_K0 12
#define A1_START 3
#define A1_PER 23
#define A1_LAG 2
#define A1_STAG 3
#define A1_ROWS 11
#define A1_DLY 8
#define H_AT 10
#define Q_AT 11
#define T_AT 99
#define A_LAG 0
#define XQ_AT 16
#define XQ_LAG 1
#define MK_AT 16
#define X_LAG 1
#define NTOUCH 3
#define TOUCH_DLY 6
#define PF_ON 0
#define LORD 0
#define PFD 0
#define BAR16_AT -1
#define WH_AT 8
#define GATE F_X1
#define ROTM 0
#define YSLEEP 6
__device__ __forceinline__ void helper_wave(const Params& P, lptr smem, int hw, int lane, int xl, int idx) {
  PAD_DECL(1)
  constexpr int LAST_ = 9;
  ST_DECL
  STAMP(0);
  const int hp = (xl & 3) * 4 + (idx & 3), rb = (xl >> 2) * 8 + (idx >> 2);
  const int hq = hp ^ QSWAP;
  const int nt = G2MAP ? ((idx & 15) ^ NTX) : 8 * (xl & 1) + ((idx & 7) ^ NTX);
  const unsigned voW = wo_voff(lane);
  const int ch = 128 * xl + 64 * (idx >> 4) + 8 * (4 * (idx & 1) + hw) + ((idx >> 1) & 7);
  unsigned dw;
  u32x4 qv[4];
  auto hburst = [&]() {
    const int i2 = 2 * (hw & 1);
    const char* hsrc = (const char*)P.ssm_k + (size_t)(128 * xl + 64 * (idx >> 4) + 8 * (4 * (idx & 1) + i2) + ((idx >> 1) & 7)) * 4096;
    const unsigned vl = lane * 16;
#pragma unroll
    for (int i = 0; i < 2; ++i) dma4(hsrc + i * 8 * 4096, (unsigned)(size_t)smem + CV_HP + (i2 + i) * CV_HS + 32, vl, vl, vl, vl);
  };
  auto x_dma = [&]() {
    const char* xs = P.ws + WS_KVT + (rb * 16 + hq) * 16384 + (hw & 1) * 8192;
    const unsigned vl = lane * 16;
#pragma unroll 1
    for (int i = 0; i < 2; ++i)
      asm volatile("s_mov_b32 m0, %0\n\ts_nop 0\n\tglobal_load_lds_dwordx4 %2, %1 sc1\n\tglobal_load_lds_dwordx4 %2, %1 offset:1024 sc1\n\t"
                   "global_load_lds_dwordx4 %2, %1 offset:2048 sc1\n\tglobal_load_lds_dwordx4 %2, %1 offset:3072 sc1"
                   ::"s"((unsigned)(size_t)smem + CV_X + (hw & 1) * 8192 + i * 4096), "s"(xs + i * 4096), "v"(vl) : "memory");
  };
  auto load_d = [&]() {
    unsigned t;
    asm volatile("s_load_dword %0, %1, 0x0\n\ts_waitcnt lgkmcnt(0)" : "=&s"(t) : "s"((const char*)P.ssm_D + (ch >> 1) * 4) : "memory");
    dw = t;
  };
  if (__builtin_expect(hw < 2, 1)) {
    const int lw = hw;
    unsigned va[4], vb[4];
    {
      const int r = lane >> 3, c = lane & 7, h = lane >> 4;
      const unsigned a0 = r * 2048 + ((c ^ h) << 4), b0 = r * 6144 + ((c ^ ((h & 1) << 1)) << 4);
#pragma unroll
      for (int j = 0; j < 4; ++j) {
        va[j] = (a0 ^ ((j & 1) << 6)) + j * (8 * 2048 - 1024);
        vb[j] = (b0 ^ ((j & 1) << 6)) + j * (8 * 6144 - 1024);
      }
    }
    const char* xa = (const char*)P.x + (rb * 128 + 64 * lw) * 2048;
    const char* wq = (const char*)P.wqkv + 32 * lw * 6144 + hq * 128;
    const char* wk = (const char*)P.wqkv + 32 * lw * 6144 + hp * 128 + 2048;
    const int ij = (idx & 3) + (idx >> 2);
    const int rot = ROTM == 0 ? 4 * (((xl & 3) + 2 * (xl >> 2) + ((idx >> 2) & 1)) & 3) : ROTM == 3 ? 8 * (ij & 1) : ROTM == 4 ? 4 * (ij & 3)
                  : ROTM == 5 ? 5 * (ij % 3) : ROTM == 6 ? 6 * (ij & 1) : ROTM == 7 ? 8 * (ij & 1) + (xl & 7) : ROTM == 8 ? 4 * (ij & 1) : ROTM == 9 ? 3 * (ij % 3) : 0;
#define LAGM 2
#define LAGN 2
    const int cls = (hp + (int)((size_t)P.wqkv >> 7)) & 7;
    const int lag = LAGM == 1 ? (cls == 3 ? LAGN : 0) : LAGM == 2 ? (cls == 3 || cls == 2 ? LAGN : 0) : LAGM == 3 ? ((idx & 3) != 0 ? LAGN : 0)
                  : LAGM == 4 ? ((idx & 3) != ((idx >> 2) & 1) ? LAGN : 0) : LAGM == 5 ? ((idx & 2) ? LAGN : 0) : 0;
    auto issue = [&](int t, int slot, auto g0_, auto g1_) {
      const int kk = (t + rot - lag) & 15;
      const unsigned sl = (unsigned)(size_t)smem + slot;
      sfor<decltype(g0_)::v, decltype(g1_)::v>([&](auto i_) {
        constexpr int ORD[3][5] = {{0, 1, 2, 3, 4}, {3, 4, 2, 0, 1}, {3, 0, 4, 1, 2}};
        constexpr int i = ORD[LORD][decltype(i_)::v];
        if constexpr (i < 2) dma4(xa + (kk * 128 + i * 32 * 2048), sl + lw * 8192 + i * 4096, va[0], va[1], va[2], va[3]);
        else dma4((i == 2 ? wq : wk) + (kk * 64 * 6144 + (i == 4 ? 2048 : 0)), sl + 16384 + (i - 2) * 8192 + lw * 4096, vb[0], vb[1], vb[2], vb[3]);
      });
    };
    TB_DECL
    issue(0, 0, IC<0>{}, IC<5>{});
    issue(1, G1_STAGE, IC<0>{}, IC<G1_K0 / 4>{});
    waitv<G1_K0>();
    TBAR(0);
    issue(1, G1_STAGE, IC<G1_K0 / 4>{}, IC<5>{});
    int slot = G1_STAGE;
#pragma unroll 1
    for (int t = 1; t < 15; ++t) {
      slot = slot + G1_STAGE == 3 * G1_STAGE ? 0 : slot + G1_STAGE;
      issue(t + 1, slot, IC<0>{}, IC<5>{});
      waitv<20>();
      TBAR(t);
    }
    if constexpr (H_AT == 10) hburst();
    {
      const unsigned st = 8 * 2048 - 1024;
      dma4((const char*)P.wo + nt * 128 + (size_t)((G2MAP ? 2 * xl + (idx >> 4) : 4 * (xl >> 1) + (idx >> 3)) * 8 + lw * 4) * 8 * 2048, (unsigned)(size_t)smem + (10 + lw * W1N) * 4096,
           voW, voW + st, voW + 2 * st, voW + 3 * st);
    }
    constexpr int NPF = 4;
    load_d();
    waitv<(H_AT == 10 ? 8 : 0) + NPF>();
    TBAR(15);
    STAMP(1);
    TB_OUT
    if constexpr (H_AT == 11) hburst();
    wo_dma(P, smem, nt, voW, lw * W1N, 2);
    if constexpr (H_AT >= 10) { waitv<8>(); lset(smem, F_WH + lw * 4, 1); }
    spin<1>([&]() { return lget(smem, F_AR) != 0; });
    wo_dma(P, smem, nt, voW, lw * W1N + 2, W1N - 2);
  } else {
    const int u = hw - 2;
    const bool leader = blockIdx.x == 0 && u == 1, follower = blockIdx.x != 0 && u == 0;
    GAS unsigned* hbp = hb_ptr(P.ws, blockIdx.x);
    unsigned h0 = 0, h1 = 0;
    if (follower) h0 = gload(hbp);
    if (u == 0) *(LDSAS u32x4*)(smem + (lane < 56 ? CV_XP + lane * 16 : CV_HP + ((lane - 56) >> 1) * CV_HS + (lane & 1) * 16)) = u32x4{0, 0, 0, 0};
    auto beat = [&]() {
      if (leader) {
#pragma unroll
        for (int i = 0; i < 4; ++i) __hip_atomic_fetch_add(hb_ptr(P.ws, i * 64 + lane), 1u, __ATOMIC_RELAXED, __HIP_MEMORY_SCOPE_AGENT);
      }
    };
    const int cg = lane & 7, rg = lane >> 3;
    const int d0 = 128 * xl + 64 * u + 8 * cg;
    const int s0 = 64 * idx + 8 * rg;
    const unsigned dump = (unsigned)(size_t)smem + X1_Z + 256 + u * 256;
    TB_DECL
    int cur = 0;
    auto advance = [&](int n) {
      sched0();
      waitl0();
#pragma unroll 1
      for (; cur < n && cur < 16; ++cur) TBAR(cur);
      sched0();
    };
#define PF2 4
    {
      const int slow = (3 - (int)(((size_t)P.wqkv >> 7) & 7) - (xl & 3) * 4) & 7;
      const int pf_n = ((idx >> 2) * 3 + ((idx & 3) - ((idx & 3) > slow ? 1 : 0))) * 76 + u * 38 + lane;
      const bool pf_on = slow < 4 && (idx & 3) != slow && lane < 38 && pf_n < 14 * 128;
      const int n2 = (pf_n % 14) * 128 + pf_n / 14;
      const unsigned pf_off = ((n2 & 127) >> 1) * 6144 + (1 + (n2 & 1)) * 2048 + ((xl & 3) * 4 + slow) * 128 + (2 + (n2 >> 7)) * 64 * 6144;
      __builtin_amdgcn_s_sleep(PF2);
      if (pf_on) touch(P.wqkv, dump, pf_off);
    }
    constexpr int A1_END = (A1_START > 3 || PF_ON == 0 ? A1_START : 3) + 22 / A1_PER + A1_LAG + (63 + A1_ROWS) / A1_ROWS;
    static_assert(A1_END + A1_STAG - 1 <= 16, "");
    static_assert(A1_END <= 16 && A1_END >= BEAT_AT + 3, "hand-shake order");
    const rsrc_t rt = mkrs(P.ws + WS_KVT, 4u << 20);
    u32x4 ld[23];
    unsigned ko = (s0 * 1024 + d0) * 2, kh = s0 == 0 ? 0x80000000u : ko - 3 * 2048;
    int vkv = (((cg >> 2) ^ QSWAP) + ((xl >> 2) * 8 + 4 * u) * 16 + (xl & 3) * 4) * 16384 + rg * 64 + (((cg ^ (2 * idx + (rg >> 2))) & 3) << 4) + idx * 512;
    asm volatile("" : "+v"(ko), "+v"(kh), "+v"(vkv));
#define SLOWG -1
    const int cls_ = (hp + (int)((size_t)P.wqkv >> 7)) & 7;
    advance(A1_START + (SLOWG >= 0 && (cls_ & 6) == 2 ? SLOWG : A1_STAG > 1 ? (idx + (xl >> 1)) % A1_STAG : 0));
    if (A1_DLY) __builtin_amdgcn_s_sleep(A1_DLY);
    STAMP(2);
    sfor<0, 23>([&](auto n_) {
      constexpr int n = decltype(n_)::v;
      if constexpr (n < 4) ld[n] = *(const u32x4*)(P.shift_k + d0 * 4 + n * 8);
      else if constexpr (n < 7) ld[n] = __builtin_amdgcn_raw_buffer_load_b128(mkrs(P.k, 4u << 20), kh + (n - 4) * 2048, 0, 0);
      else if constexpr (n < 15) ld[n] = *(const u32x4*)((const char*)P.k + (n - 7) * 2048 + ko);
      else ld[n] = *(const u32x4*)((const char*)P.v + (n - 15) * 2048 + ko);
      if constexpr (n % A1_PER == A1_PER - 1 && n != 22) {
        advance(cur + 1);
        if (A1_DLY) __builtin_amdgcn_s_sleep(A1_DLY);
      }
    });
    load_d();
    auto tburst = [&]() {
      if (lane < 32) touch(P.wo, dump, (blockIdx.x * 64 + u * 32 + lane) * 128);
    };
    if constexpr (H_AT < 0) hburst();
    if constexpr (T_AT < 0) tburst();
    advance(cur + A1_LAG);
    sfor<0, 8>([&](auto c_) {
      constexpr int c = decltype(c_)::v;
      const unsigned wa = ld[c >> 1][(c & 1) * 2], wb = ld[c >> 1][(c & 1) * 2 + 1];
      const float w0 = bflo(wa), w1 = bfhi(wa), w2 = bflo(wb), w3 = bfhi(wb);
      float kf[11], r[8];
#pragma unroll
      for (int i = 0; i < 11; ++i) {
        kf[i] = (c & 1) ? bfhi(ld[4 + i][c >> 1]) : bflo(ld[4 + i][c >> 1]);
      }
#pragma unroll
      for (int i = 0; i < 8; ++i) {
        float ks = kf[i] * w3;
        ks = __builtin_fmaf(kf[i + 1], w2, ks);
        ks = __builtin_fmaf(kf[i + 2], w1, ks);
        ks = __builtin_fmaf(kf[i + 3], w0, ks);
        r[i] = ks * ((c & 1) ? bfhi(ld[15 + i][c >> 1]) : bflo(ld[15 + i][c >> 1]));
        if ((c * 8 + i + 1) % A1_ROWS == 0 && i != 7) {
#pragma unroll
          for (int j = 0; j <= i; ++j) pin(r[j]);
          advance(cur + 1);
        }
      }
      st_sc1(u32x4{pack2(r[0], r[1]), pack2(r[2], r[3]), pack2(r[4], r[5]), pack2(r[6], r[7])}, rt, vkv, ((c >> 1) * 16 + 2 * (c & 1)) * 16384);
      if constexpr (c == 5) { if (follower) h1 = gload(hbp); }
      if constexpr (c == 7) {
        if (follower) {
          if (__builtin_amdgcn_readfirstlane(h1) == __builtin_amdgcn_readfirstlane(h0))
            spin<8>([&]() { return __builtin_amdgcn_readfirstlane(gload(hbp)) != __builtin_amdgcn_readfirstlane(h0); });
          if (lane == 0) __hip_atomic_fetch_add(ack_ptr(P.ws, xl), 1u, __ATOMIC_RELAXED, __HIP_MEMORY_SCOPE_AGENT);
        }
      }
      {
        const u32x4 mk = {MARK, MARK, MARK, MARK};
        const rsrc_t rp = mkrs(P.ws + WS_KVP, 1024 * KVP_SLOT);
        if constexpr (c == 1 || c == 3) st_sc1(mk, rp, lane * 16, ((hp * 4 + u + (c >> 1) * 2) * 16 + rb) * KVP_SLOT);
        if constexpr (c == 5) { if (lane < 16) st_sc1(mk, rp, (lane & 7) * 16 + (lane >> 3) * 32 * KVP_SLOT, ((hp * 4 + u) * 16 + rb) * KVP_SLOT + 1024); }
      }
      if constexpr (c == H_AT) hburst();
      if constexpr (c == T_AT) tburst();
      if constexpr (T_AT == 9) { if (c == (idx & 7)) tburst(); }
      if constexpr (H_AT == 9) { if (c == ((idx >> 2) & 7)) hburst(); }
      if constexpr ((c * 8 + 8) % A1_ROWS == 0 || c == 7) advance(cur + 1);
    });
    advance(cur + A_LAG);
    waitv<0>();
    gpost(P.ws, MB_A, (u * 16 + lane) * 8 + xl, 16, lane);
    if constexpr (H_AT == 8) hburst();
    if constexpr (T_AT == 8) tburst();
    STAMP(3);
    const char* abx = P.ws + WS_CNT + blockIdx.x * MB_STRIDE + MB_A * 4;
    constexpr bool xearly = false;
    if (xearly) {
      x_dma();
    }
    advance(15);
    if (!xearly && u == 0) poll_prime<0>(abx);
    advance(16);
    STAMP(1);
    TB_OUT
    if (leader) {
      GAS unsigned* ap = ack_ptr(P.ws, lane & 7);
      spin<16>([&]() {
        if (__builtin_amdgcn_ballot_w64(gload(ap) < 32u) == 0) return true;
        beat();
        return false;
      });
    }
    if (!xearly) {
      if (u == 0) poll_prime<1>(abx);
      group_wait<1, false>(smem, F_AR, u == 0, abx, 32);
      x_dma();
    } else if (u == 0) lset(smem, F_AR, 1);
    waitv<0>();
    lset(smem, F_HR + u * 4, 1);
    if constexpr (H_AT < 10) lset(smem, F_WH + u * 4, 1);
  }
  const float Dv = u2f((ch & 1) ? (dw & 0xffff0000u) : (dw << 16));
  STAMP(4);
  {
    const int l15 = lane & 15, g = lane >> 4;
    const int q4 = l15 >> 2, p4 = lane & 3;
    lptr xb[2][2];
#pragma unroll
    for (int par = 0; par < 2; ++par)
#pragma unroll
      for (int hf = 0; hf < 2; ++hf) {
        const int t4 = 28 + 16 * par + 3 + 4 * p4 - 8 * g - 4 * hf - q4;
        const int s8 = t4 >> 1;
        xb[par][hf] = smem + CV_XP + s8 * 64 + ((hw ^ (((s8 + 2) >> 2) & 3)) << 4) + (t4 & 1) * 8;
      }
    lptr hb = smem + CV_HP + hw * CV_HS + 32 - 24 + 8 * p4 + 64 * g + 8 * q4;
    const rsrc_t ry = mkrs(P.ws + WS_Y2T, 4u << 20);
    int vy = ch * 4096 + lane * 16;
    asm volatile("" : "+v"(xb[0][0]), "+v"(xb[0][1]), "+v"(xb[1][0]), "+v"(xb[1][1]), "+v"(hb), "+v"(vy));
    __builtin_amdgcn_s_setprio(H_PRIO);
    spin<1>([&]() { return lmin2(smem, F_HR) != 0 && lmin2(smem, F_WH) != 0; });
    if (lane == 0) {
      volatile LDSAS u16* h0 = (volatile LDSAS u16*)(smem + CV_HP + hw * CV_HS + 32);
      *h0 = (u16)pack2(bflo(*h0) + Dv, 0.f);
    }
    const char* wsrc = (const char*)P.wo + nt * 128 + (size_t)((hw >> 1) * 80 + (hw & 1) * 20) * 8 * 2048;
    const unsigned wdst = (unsigned)(size_t)smem + ((hw >> 1) * 80 + (hw & 1) * 20) * 1024;
    const int gq = 8 * idx + (lane >> 3);
    unsigned qo = (((gq & 63) + 512 * (gq >> 6) + 256 * (hw >> 1)) * 1024 + 128 * xl + 64 * (hw & 1) + 8 * (lane & 7)) * 2;
    unsigned fb = (unsigned)(size_t)smem + L_FLG + hw * 4;
    asm volatile("" : "+v"(qo), "+v"(fb));
    spin<1>([&]() { return lmin4(smem, GATE) != 0; });
    if (hw < 2) { waitv<0>(); lset(smem, F_HR + 8 + hw * 4, 1); }
    STAMP(5);
    asm volatile(CONV_ASM
                 : "=&v"(qv[0]), "=&v"(qv[1]), "=&v"(qv[2]), "=&v"(qv[3])
                 : "v"(xb[0][0]), "v"(xb[0][1]), "v"(xb[1][0]), "v"(xb[1][1]), "v"(hb), "v"(vy), "v"(voW), "v"(qo), "v"(fb),
                   "s"(ry), "s"(wsrc), "s"(wdst), "s"(P.q)
                 : "memory", "vcc", "scc", "s40", "s41", "s42", CONV_CLOBBERS);
    STAMP(6);
  }
  {
    const int u = hw & 1, jh = hw >> 1;
    const int cg = lane & 7, rg = lane >> 3;
    const int gq = 8 * idx + rg;
    const int sq = (gq & 63) + 512 * (gq >> 6) + 256 * jh;
    spin<1>([&]() { return lmin4(smem, F_CVD) != 0; });
#pragma unroll
    for (int i = 0; i < 2; ++i)
      dma16s((const char*)P.wo + nt * 128 + (AH(30) * 4 + hw * 2 + i) * 16384, (unsigned)(size_t)smem + (120 + hw * 2 + i) * 1024, voW);
    waitv<0>();
    lset(smem, F_WL + hw * 4, 1);
    gsignal4(P.ws, smem, F_NY, MB_Y, lane * 8 + xl, 32, lane);
    STAMP(7);
    __builtin_amdgcn_s_setprio(A3_PRIO);
    float qf[4][8], yf[4][8];
    asm volatile("" : "+v"(qv[0]), "+v"(qv[1]), "+v"(qv[2]), "+v"(qv[3]));
#pragma unroll
    for (int j = 0; j < 4; ++j)
#pragma unroll
      for (int c = 0; c < 8; ++c) { qf[j][c] = (c & 1) ? bfhi(qv[j][c >> 1]) : bflo(qv[j][c >> 1]); yf[j][c] = 0.f; pin(qf[j][c]); pin(yf[j][c]); }
    const char* yb = P.ws + WS_Y2T + (128 * xl + 32 * hw) * 4096 + idx * 128;
    unsigned vy[4];
#pragma unroll
    for (int i = 0; i < 4; ++i) vy[i] = rg * 4096 + ((cg ^ ((4 * hw + i) & 7)) << 4) + i * (8 * 4096 - 1024);
    const unsigned yl = (unsigned)(size_t)smem + L_YT + hw * 4096;
    unsigned tr = (unsigned)(size_t)smem + L_YT + (64 * u + 8 * cg) * 128 + ((rg ^ cg) << 4) + jh * 8;
    unsigned oo = (sq * 2048 + 128 * xl + 64 * u + 8 * cg) * 2;
    asm volatile("" : "+s"(yb), "+v"(vy[0]), "+v"(vy[1]), "+v"(vy[2]), "+v"(vy[3]), "+v"(tr), "+v"(oo));
    group_wait<2, true>(smem, F_YR, hw == 0, P.ws + WS_CNT + blockIdx.x * MB_STRIDE + MB_Y * 4, 32);
    STAMP(8);
    asm volatile("s_mov_b32 m0, %0\n\ts_nop 0\n\tglobal_load_lds_dwordx4 %2, %1 sc1\n\tglobal_load_lds_dwordx4 %3, %1 offset:1024 sc1\n\t"
                 "global_load_lds_dwordx4 %4, %1 offset:2048 sc1\n\tglobal_load_lds_dwordx4 %5, %1 offset:3072 sc1\n\ts_waitcnt vmcnt(0)"
                 ::"s"(yl), "s"(yb), "v"(vy[0]), "v"(vy[1]), "v"(vy[2]), "v"(vy[3]) : "memory");
    lset(smem, F_YL + hw * 4, 1);
    spin<1>([&]() { return lmin2(smem, F_YL + u * 8) != 0; });
    sfor<0, 4>([&](auto j_) {
      constexpr int j = decltype(j_)::v;
      float* y = yf[j];
      const unsigned a = tr;
      asm volatile("ds_read_u16_d16_hi %0, %8 offset:%9\n\tds_read_u16_d16_hi %1, %8 offset:%9+128\n\tds_read_u16_d16_hi %2, %8 offset:%9+256\n\t"
                   "ds_read_u16_d16_hi %3, %8 offset:%9+384\n\tds_read_u16_d16_hi %4, %8 offset:%9+512\n\tds_read_u16_d16_hi %5, %8 offset:%9+640\n\t"
                   "ds_read_u16_d16_hi %6, %8 offset:%9+768\n\tds_read_u16_d16_hi %7, %8 offset:%9+896"
                   : "+v"(y[0]), "+v"(y[1]), "+v"(y[2]), "+v"(y[3]), "+v"(y[4]), "+v"(y[5]), "+v"(y[6]), "+v"(y[7]) : "v"(a), "i"(2 * j) : "memory");
      if constexpr (j > 0) {
        float* z = yf[j - 1];
        asm volatile("s_waitcnt lgkmcnt(8)" : "+v"(z[0]), "+v"(z[1]), "+v"(z[2]), "+v"(z[3]), "+v"(z[4]), "+v"(z[5]), "+v"(z[6]), "+v"(z[7])::"memory");
        st_out<OUTAUX3>(u32x4{pack2(qf[j - 1][0] * z[0], qf[j - 1][1] * z[1]), pack2(qf[j - 1][2] * z[2], qf[j - 1][3] * z[3]),
                              pack2(qf[j - 1][4] * z[4], qf[j - 1][5] * z[5]), pack2(qf[j - 1][6] * z[6], qf[j - 1][7] * z[7])}, P.out, oo + (j - 1) * 64 * 4096);
        sched0();
      }
    });
    {
      float* z = yf[3];
      asm volatile("s_waitcnt lgkmcnt(0)" : "+v"(z[0]), "+v"(z[1]), "+v"(z[2]), "+v"(z[3]), "+v"(z[4]), "+v"(z[5]), "+v"(z[6]), "+v"(z[7])::"memory");
      st_out<OUTAUX3>(u32x4{pack2(qf[3][0] * z[0], qf[3][1] * z[1]), pack2(qf[3][2] * z[2], qf[3][3] * z[3]),
                            pack2(qf[3][4] * z[4], qf[3][5] * z[5]), pack2(qf[3][6] * z[6], qf[3][7] * z[7])}, P.out, oo + 3 * 64 * 4096);
    }
  }
  STAMP(9);
  ST_OUT;
  PAD_END;
}

__global__ __launch_bounds__(512, 2) void fused_kernel(Params P) {
  extern __shared__ __attribute__((aligned(16))) char smem_[];
  lptr smem = (lptr)smem_;
  const void* pc0;
  asm volatile("s_getpc_b64 %0" : "=s"(pc0));
  const int lane = threadIdx.x & 63, wid = __builtin_amdgcn_readfirstlane(threadIdx.x >> 6);
  const int xl = blockIdx.x & 7, idx = blockIdx.x >> 3;
  if (__builtin_expect(wid >= 4, 1)) helper_wave(P, smem, wid - 4, lane, xl, idx);
  else compute_wave(P, smem, wid, lane, xl, idx, pc0);
}

#if defined(__HIP_DEVICE_COMPILE__)
#pragma clang attribute pop
#endif

extern "C" void kernel_launch(void* const* d_in, const int* in_sizes, int n_in,
                              void* d_out, int out_size, void* d_ws, size_t ws_size,
                              hipStream_t stream) {
  Params P;
  P.q = (const u16*)d_in[0];
  P.k = (const u16*)d_in[1];
  P.v = (const u16*)d_in[2];
  P.shift_k = (const u16*)d_in[3];
  P.ssm_k = (const u16*)d_in[4];
  P.ssm_D = (const u16*)d_in[5];
  P.x = (const u16*)d_in[6];
  P.wqkv = (const u16*)d_in[7];
  P.wo = (const u16*)d_in[8];
  P.ws = (char*)d_ws;
  P.out = (u16*)d_out;
  (void)hipFuncSetAttribute((const void*)fused_kernel, hipFuncAttributeMaxDynamicSharedMemorySize, SMEM_BYTES);
  fused_kernel<<<256, 512, SMEM_BYTES, stream>>>(P);
}
```

```cpp
#include <hip/hip_runtime.h>
#include <stdint.h>

typedef __attribute__((ext_vector_type(8))) __bf16 bf16x8;
typedef __attribute__((ext_vector_type(4))) __bf16 bf16x4;
typedef __attribute__((ext_vector_type(2))) __bf16 bf16x2;
typedef __attribute__((ext_vector_type(4))) float f32x4;
typedef __attribute__((ext_vector_type(2))) float f32x2;
typedef __attribute__((ext_vector_type(4))) unsigned int u32x4;
typedef __attribute__((ext_vector_type(2))) unsigned int u32x2;
typedef unsigned short u16;
#define LDSAS __attribute__((address_space(3)))
#define GAS __attribute__((address_space(1)))
typedef LDSAS char* lptr;
typedef __amdgpu_buffer_rsrc_t rsrc_t;

#define WS_KVT   (0u)
#define WS_Y2T   (8u << 20)
#define WS_KVP   (12u << 20)
#define KVP_SLOT 1152
#define WS_CNT   (13u << 20 | 512u << 10)
#define WS_USED  (14u << 20)
#define WS_STAMP (20u << 20)
#define WS_SPAN (14u << 20)
#define MB_P 0
#define MB_O 1
#define MB_O2 2
#define MB_A 4096
#define MB_Y 4097
#define MB_STRIDE 64
#define MARK 0xFFBFFFBFu

#define G1_STAGE 40960
#define CV_XP 122880
#define CV_X (CV_XP + 896)
#define CV_HP (CV_X + 16384)
#define CV_HS 4128
#define L_SCR 159360
#define L_FLG 163456
#define L_YT 132096
#define F_X1 (L_FLG + 0)
#define F_WC (L_FLG + 144)
#define F_YL (L_FLG + 160)
#define F_T15 (L_FLG + 128)
#define F_X2 (L_FLG + 16)
#define F_HR (L_FLG + 32)
#define F_CVD (L_FLG + 48)
#define F_WH (L_FLG + 64)
#define F_WL (L_FLG + 80)
#define F_NP (L_FLG + 96)
#define F_NO (L_FLG + 100)
#define F_NY (L_FLG + 104)
#define F_PR (L_FLG + 108)
#define F_OR (L_FLG + 112)
#define F_YR (L_FLG + 116)
#define F_AR (L_FLG + 120)
#define SMEM_BYTES 163840
#define X1_Z 156672

#define CODE_BYTES 31500
#define SPIN_LIMIT (1 << 17)

#define ST_DECL
#define ST_OUT
#define STAMP(slot) do {} while (0)
#define CNT_SLOW(k)
#define PAD_DECL(role)
#define PAD_END
#define TB_DECL
#define TBAR(t) bar()
#define TB_OUT
struct Params {
  const u16 *q, *k, *v, *shift_k, *ssm_k, *ssm_D, *x, *wqkv, *wo;
  char* ws;
  u16* out;
};

#if defined(__HIP_DEVICE_COMPILE__)
#pragma clang attribute push(__attribute__((target("no-packed-fp32-ops"))), apply_to = function)
#endif
__device__ __forceinline__ float u2f(unsigned x) { return __builtin_bit_cast(float, x); }
__device__ __forceinline__ float i2f(int x) { return __builtin_bit_cast(float, x); }
__device__ __forceinline__ int f2i(float x) { return __builtin_bit_cast(int, x); }
template <int N> struct IC { static constexpr int v = N; };
template <int B, int E, class F> __device__ __forceinline__ void sfor(F f) {
  if constexpr (B < E) { f(IC<B>{}); sfor<B + 1, E>(f); }
}
template <int N> __device__ __forceinline__ void waitv() { asm volatile("s_waitcnt vmcnt(%0)" ::"i"(N) : "memory"); }
__device__ __forceinline__ void waitl0() { asm volatile("s_waitcnt lgkmcnt(0)" ::: "memory"); }
__device__ __forceinline__ void wait_all() { __builtin_amdgcn_s_waitcnt(0x0070); asm volatile("s_waitcnt vmcnt(0) lgkmcnt(0)" ::: "memory"); }
__device__ __forceinline__ void bar() { asm volatile("s_barrier" ::: "memory"); }
__device__ __forceinline__ void sched0() { __builtin_amdgcn_sched_barrier(0); }
__device__ __forceinline__ void cfence() { asm volatile("" ::: "memory"); }

__device__ __forceinline__ rsrc_t mkrs(const void* p, unsigned bytes) {
  return __builtin_amdgcn_make_buffer_rsrc((void*)p, 0, bytes, 0x00020000);
}
__device__ __forceinline__ void dma16(const void* base, lptr dst, unsigned voff, unsigned soff) {
  __builtin_amdgcn_global_load_lds((const GAS void*)((const char*)base + (size_t)soff + voff), (LDSAS void*)dst, 16, 0, 0);
}
__device__ __forceinline__ void dma16s(const void* sbase, unsigned lds_addr, unsigned voff) {
  asm volatile("s_mov_b32 m0, %0\n\ts_nop 0\n\tglobal_load_lds_dwordx4 %1, %2" ::"s"(lds_addr), "v"(voff), "s"(sbase) : "memory");
}
__device__ __forceinline__ void dma4(const void* sbase, unsigned lds_addr, unsigned v0, unsigned v1, unsigned v2, unsigned v3) {
  asm volatile("s_mov_b32 m0, %0\n\ts_nop 0\n\tglobal_load_lds_dwordx4 %2, %1\n\tglobal_load_lds_dwordx4 %3, %1 offset:1024\n\t"
               "global_load_lds_dwordx4 %4, %1 offset:2048\n\tglobal_load_lds_dwordx4 %5, %1 offset:3072"
               ::"s"(lds_addr), "s"(sbase), "v"(v0), "v"(v1), "v"(v2), "v"(v3) : "memory");
}
__device__ __forceinline__ void touch(const void* sbase, unsigned lds_addr, unsigned voff) {
  asm volatile("s_mov_b32 m0, %0\n\ts_nop 0\n\tglobal_load_lds_dword %2, %1" ::"s"(lds_addr), "s"(sbase), "v"(voff) : "memory");
}
__device__ __forceinline__ u32x4 ld_sc1(rsrc_t rs, int voff, int soff) { return __builtin_amdgcn_raw_buffer_load_b128(rs, voff, soff, 16); }
__device__ __forceinline__ void st_sc1(u32x4 v, rsrc_t rs, int voff, int soff) { __builtin_amdgcn_raw_buffer_store_b128(v, rs, voff + soff, 0, 16); }
#define OUTAUX 16
#define OUTAUX3 OUTAUX
template <int AUX> __device__ __forceinline__ void st_out(u32x4 v, void* out, unsigned off) { __builtin_amdgcn_raw_buffer_store_b128(v, mkrs(out, 8u << 20), off, 0, AUX); }
__device__ __forceinline__ float dot2(unsigned a, unsigned b, float c) {
  return __builtin_amdgcn_fdot2_f32_bf16(__builtin_bit_cast(bf16x2, a), __builtin_bit_cast(bf16x2, b), c, false);
}
__device__ __forceinline__ void pin(float& x) { asm volatile("" : "+v"(x)); }
__device__ __forceinline__ unsigned pack2(float lo, float hi) {
  f32x2 v = {lo, hi};
  return __builtin_bit_cast(unsigned, __builtin_convertvector(v, bf16x2));
}
__device__ __forceinline__ float bflo(unsigned w) { return u2f(w << 16); }
__device__ __forceinline__ float bfhi(unsigned w) { return u2f(w & 0xffff0000u); }
__device__ __forceinline__ float phi_f(float t1) {
  const float e = __builtin_amdgcn_exp2f(__builtin_fmaf(t1, 1.44269504088896f, -1.44269504088896f));
  return __builtin_amdgcn_fmed3f(e, t1, 1.f) + 1e-6f;
}
__device__ __forceinline__ bf16x8 trread2(lptr p0, lptr p1) {
  bf16x4 lo = __builtin_amdgcn_ds_read_tr16_b64_v4bf16((LDSAS bf16x4*)p0);
  bf16x4 hi = __builtin_amdgcn_ds_read_tr16_b64_v4bf16((LDSAS bf16x4*)p1);
  return __builtin_shufflevector(lo, hi, 0, 1, 2, 3, 4, 5, 6, 7);
}
#define MFMA16(a, b, c) __builtin_amdgcn_mfma_f32_16x16x32_bf16((a), (b), (c), 0, 0, 0)
#define BC8(x) __builtin_bit_cast(bf16x8, (x))
__device__ __forceinline__ float shr1(float v) {
  return i2f(__builtin_amdgcn_update_dpp(0, f2i(v), 0x138, 0xf, 0xf, true));
}
__device__ __forceinline__ float fsel(bool c, float a, float b) { return c ? a : b; }
__device__ __forceinline__ float ror1(float v) {
  return i2f(__builtin_amdgcn_update_dpp(0, f2i(v), 0x13C, 0xf, 0xf, true));
}
template <int S> __device__ __forceinline__ float row_ror(float v) {
  return i2f(__builtin_amdgcn_update_dpp(0, f2i(v), 0x120 + S, 0xf, 0xf, true));
}

__device__ __forceinline__ void lset(lptr smem, int off, int val) {
  asm volatile("s_mov_b64 exec, 1\n\tds_write_b32 %0, %1\n\ts_mov_b64 exec, -1" ::"v"((unsigned)(size_t)smem + off), "v"(val) : "memory");
}
__device__ __forceinline__ int lmin2(lptr smem, int off) {
  const u32x2 v = *(volatile LDSAS u32x2*)(smem + off);
  return __builtin_amdgcn_readfirstlane((int)(v[0] < v[1] ? v[0] : v[1]));
}
__device__ __forceinline__ int lmin4(lptr smem, int off) {
  const u32x4 v = *(volatile LDSAS u32x4*)(smem + off);
  const unsigned a = v[0] < v[1] ? v[0] : v[1], b = v[2] < v[3] ? v[2] : v[3];
  return __builtin_amdgcn_readfirstlane((int)(a < b ? a : b));
}
__device__ __forceinline__ int lget(lptr smem, int off) {
  return __builtin_amdgcn_readfirstlane(*(volatile LDSAS int*)(smem + off));
}
template <int SLEEP, class F> __device__ __forceinline__ void spin(F ready) {
#pragma unroll 1
  for (int it = 0; it < SPIN_LIMIT; ++it) {
    if (ready()) break;
    if (SLEEP) __builtin_amdgcn_s_sleep(SLEEP);
  }
  cfence();
}
__device__ __forceinline__ GAS unsigned* cnt_ptr(char* ws, int wg) { return (GAS unsigned*)(ws + WS_CNT + wg * MB_STRIDE); }
__device__ __forceinline__ GAS unsigned* ack_ptr(char* ws, int i) { return (GAS unsigned*)(ws + WS_CNT + 32768 + i * 128); }
__device__ __forceinline__ GAS unsigned* hb_ptr(char* ws, int wg) { return (GAS unsigned*)(ws + WS_CNT + 32768 + 1024 + wg * 4); }
__device__ __forceinline__ unsigned gload(GAS unsigned* p) { return __hip_atomic_load(p, __ATOMIC_RELAXED, __HIP_MEMORY_SCOPE_AGENT); }
__device__ __forceinline__ void gpost(char* ws, int kind, int wg, int n, int lane) {
  if (lane < n) __hip_atomic_fetch_add(cnt_ptr(ws, wg) + kind, 1u, __ATOMIC_RELAXED, __HIP_MEMORY_SCOPE_AGENT);
}
__device__ __forceinline__ void gsignal4(char* ws, lptr smem, int cnt, int kind, int wg, int n, int lane) {
  unsigned old;
  asm volatile("s_mov_b64 exec, 1\n\tds_add_rtn_u32 %0, %1, %2\n\ts_mov_b64 exec, -1\n\ts_waitcnt lgkmcnt(0)"
               : "=&v"(old) : "v"((unsigned)(size_t)smem + cnt), "v"(1u) : "memory");
  if (__builtin_amdgcn_readfirstlane(old) == 3) gpost(ws, kind, wg, n, lane);
}
#define PSLEEP 4
__device__ __forceinline__ void gwait(char* ws, int kind, unsigned target) {
  GAS unsigned* p = cnt_ptr(ws, blockIdx.x) + kind;
  spin<PSLEEP>([&]() { return __builtin_amdgcn_readfirstlane(gload(p)) >= target; });
}

#define LDG(dst, voff, base, off) asm volatile("global_load_dwordx4 %0, %1, %2 offset:" #off " sc1" : "=v"(dst) : "v"(voff), "s"(base) : "memory")

struct G1Frag { bf16x8 a[4], b[6]; };

template <int N> __device__ __forceinline__ void post(const char* base, unsigned voff) {
  asm volatile("s_mov_b64 exec, %3\n\tglobal_atomic_add %0, %1, %2\n\ts_mov_b64 exec, -1" ::"v"(voff), "v"(1u), "s"(base), "i"(N == 16 ? 0xffffll : 0xffffffffll) : "memory");
}
__device__ __forceinline__ unsigned arrive(lptr smem, int flag, int cnt) {
  unsigned old;
  asm volatile("s_mov_b64 exec, 1\n\tds_write_b32 %1, %3\n\tds_add_rtn_u32 %0, %2, %3\n\ts_mov_b64 exec, -1"
               : "=&v"(old) : "v"((unsigned)(size_t)smem + flag), "v"((unsigned)(size_t)smem + cnt), "v"(1u) : "memory");
  return old;
}
__device__ __forceinline__ bool was_last(unsigned old) {
  asm volatile("s_waitcnt lgkmcnt(0)" : "+v"(old)::"memory");
  return __builtin_amdgcn_readfirstlane(old) == 3;
}
#define PSL 3
#define POLL1(NEW, OLD) "global_atomic_add " NEW ", v252, v252, %2 sc0\n\ts_waitcnt vmcnt(2)\n\tv_readfirstlane_b32 %1, " OLD "\n\t" \
                        "s_cmp_ge_u32 %1, %3\n\ts_cbranch_scc1 2f\n\ts_sleep %5\n\t"
template <int R> __device__ __forceinline__ void poll_prime(const char* base) {
  if constexpr (R == 0)
    asm volatile("s_mov_b64 exec, 1\n\tv_mov_b32 v252, 0\n\tglobal_atomic_add v253, v252, v252, %0 sc0\n\ts_mov_b64 exec, -1" ::"s"(base) : "memory", "v252", "v253");
  else
    asm volatile("s_mov_b64 exec, 1\n\tv_mov_b32 v252, 0\n\tglobal_atomic_add v254, v252, v252, %0 sc0\n\ts_mov_b64 exec, -1" ::"s"(base) : "memory", "v252", "v254");
}
__device__ __forceinline__ void poll_loop(lptr smem, int flag, const char* base, unsigned target) {
  int it = SPIN_LIMIT;
  unsigned t;
  asm volatile("s_mov_b64 exec, 1\n\tv_mov_b32 v252, 0\n"
               "1:\n\t" POLL1("v255", "v253") POLL1("v253", "v254") POLL1("v254", "v255")
               "s_sub_u32 %0, %0, 1\n\ts_cbranch_scc0 1b\n"
               "2:\n\tv_mov_b32 v252, 1\n\tds_write_b32 %4, v252\n\ts_mov_b64 exec, -1"
               : "+s"(it), "=&s"(t) : "s"(base), "s"(target), "v"((unsigned)(size_t)smem + flag), "i"(PSL)
               : "memory", "scc", "v252", "v253", "v254", "v255");
}
template <int SLEEP, bool PRIME> __device__ __forceinline__ void group_wait(lptr smem, int flag, bool poller, const char* base, unsigned target) {
  if (poller) {
    if (PRIME) { poll_prime<0>(base); __builtin_amdgcn_s_sleep(PSL); poll_prime<1>(base); __builtin_amdgcn_s_sleep(PSL); }
    poll_loop(smem, flag, base, target);
  } else spin<SLEEP>([&]() { return lget(smem, flag) != 0; });
}
#define LDG2(dst, voff, base, off) asm volatile("global_load_dwordx2 %0, %1, %2 offset:" #off " sc1" : "=v"(dst) : "v"(voff), "s"(base) : "memory")
template <int OFF> __device__ __forceinline__ void ldg_sc1(u32x4& dst, unsigned voff, const char* base) {
  asm volatile("global_load_dwordx4 %0, %1, %2 offset:%3 sc1" : "=v"(dst) : "v"(voff), "s"(base), "i"(OFF) : "memory");
}
__device__ __forceinline__ bool any_lane(bool c) { return __builtin_amdgcn_ballot_w64(c) != 0; }
__device__ __forceinline__ bool has_mark(u32x4 v) { return (v[0] == MARK) | (v[1] == MARK) | (v[2] == MARK) | (v[3] == MARK); }
__device__ __forceinline__ unsigned wo_voff(int lane) { return (lane >> 3) * 2048 + (((lane & 7) ^ (((lane >> 4) & 3) << 1)) << 4); }
#define HD(h) ((h) < 30 ? ((h) + 10) % 30 : (h))
#define AH(h) ((int)((0x51764320u >> (4 * ((h) >> 2))) & 15u) * 4 + ((h) & 3))
__device__ __forceinline__ void wo_dma(const Params& P, lptr smem, int nt, unsigned voW, int h0, int n) {
  const unsigned st = 8 * 2048 - 1024;
#pragma unroll 1
  for (int h = h0; h < h0 + n; ++h)
    dma4((const char*)P.wo + nt * 128 + AH(h) * 65536, (unsigned)(size_t)smem + (h < 20 ? h + 10 : h - 20) * 4096, voW, voW + st, voW + 2 * st, voW + 3 * st);
}
#define MK_MODE 0
#define MK_DELAY 10
#define ZERO_DELAY 16
#define BEAT_AT 3
#define G2MAP 0
#define NTX 2
#define QSWAP 1
#define OD 16
#define P_POST 1
#define O_POST 2
#define O2_AT (24 - OD / 2 - (OD >= 16 ? 6 : OD / 2 - 1))
#define PQ_PRE 2
#define W2_POLLER 0
#define W3_EARLY 0
#define SNAKE 1
#define W1N 5
#define W1_GATE 1
#define CSL 0
#define A3_PRIO 0
#define CHK_TO 12
#define G2_PRIO 1
#define H_PRIO 2
#define C_PRIO 3

__device__ __forceinline__ void compute_wave(const Params& P, lptr smem, int wid, int lane, int xl, int idx, const void* pc0) {
  const int l15 = lane & 15, g = lane >> 4;
  const int q4 = l15 >> 2, p4 = lane & 3;
  const int hp = (xl & 3) * 4 + (idx & 3), rb = (xl >> 2) * 8 + (idx >> 2);
  const int hq = hp ^ QSWAP;
  const int nt = G2MAP ? ((idx & 15) ^ NTX) : 8 * (xl & 1) + ((idx & 7) ^ NTX);
  const int wr = wid >> 1, wh = wid & 1;
  PAD_DECL(0)
  constexpr int LAST_ = 10;
  ST_DECL
  STAMP(0);
  if (wid == 0) *(LDSAS int*)(smem + L_FLG + lane * 4) = 0;
  if (blockIdx.x == 0 && wid == 1 && lane == 0) { unsigned long long t = __builtin_amdgcn_s_memrealtime(); __hip_atomic_store((unsigned long long*)(P.ws + (30u << 20)), t, __ATOMIC_RELAXED, __HIP_MEMORY_SCOPE_AGENT); }
  if (wid == 1 && lane < 9 && (idx * 9 + lane) * 128 < CODE_BYTES) touch(pc0, (unsigned)(size_t)smem + L_SCR + 512, (idx * 9 + lane) * 128);
  const char* cntb = P.ws + WS_CNT;
  const char* mbx = cntb + blockIdx.x * MB_STRIDE;
  if (blockIdx.x == 0) {
    const rsrc_t rc = mkrs(cntb, 32768 + 1024);
    __builtin_amdgcn_s_sleep(ZERO_DELAY);
#pragma unroll 1
    for (int i = 0; i < 8; ++i) st_sc1(u32x4{0u, 0u, 0u, 0u}, rc, lane * 16, (wid * 8 + i) * 1024);
    if (wid == 3) st_sc1(u32x4{lane == 0 ? 1u : 0u, 0u, 0u, 0u}, rc, lane * 16, 32768);
  }
  u32x4 pqf[4], pqs[4], whv;
  float pq[4][8];
  f32x4 s0, s1;
  bool oready = false;
  int vst = lane * 16 + (rb * 16 + hq) * 16384 + (wr * 4 + wh) * 2048;
  int vst2 = vst + 4096;
  unsigned vpostO = (G2MAP ? (16 * (rb & 1) + (lane & 15)) * 8 + (rb >> 1) : (8 * (rb & 3) + (lane & 7)) * 8 + 2 * (rb >> 2) + ((lane >> 3) & 1)) * MB_STRIDE;
  lptr xp = smem + L_SCR + (wid ^ 2) * 1024 + lane * 16;
  lptr k0 = smem + L_SCR + (wh + 2) * 1024 + lane * 16 + wr * 8, k1 = smem + L_SCR + wh * 1024 + lane * 16 + (wr ^ 1) * 8;
  lptr k0x = (lptr)((unsigned)(size_t)k0 ^ 8u), k1x = (lptr)((unsigned)(size_t)k1 ^ 8u);
  const rsrc_t ro = mkrs(P.ws + WS_KVT, 4u << 20);
  asm volatile("" : "+v"(vst), "+v"(vst2), "+v"(vpostO), "+v"(xp), "+v"(k0), "+v"(k1), "+v"(k0x), "+v"(k1x));
  unsigned voW = wo_voff(lane);
  asm volatile("" : "+v"(voW));
  {
    f32x4 acc[6][4];
#pragma unroll
    for (int i = 0; i < 6; ++i)
#pragma unroll
      for (int j = 0; j < 4; ++j) { const float c0 = i < 4 ? 1.f : 0.f; acc[i][j] = f32x4{c0, c0, c0, c0}; asm volatile("" : "+v"(acc[i][j])); }
    const int abase = (64 * wr + l15) * 128 + ((g ^ (l15 >> 1)) << 4);
    const int bbase = (16384 + (8 * g + q4) * 128 + (((4 * wh + (p4 >> 1)) ^ (((q4 >> 1) | ((g & 1) << 1)) << 1)) << 4) + (p4 & 1) * 8) ^ (wr << 5);
    lptr pa0 = smem + abase, pa1 = smem + (abase ^ 64), pb0 = smem + bbase, pb1 = smem + (bbase ^ 32);
    auto ld = [&](G1Frag& f, int ks) {
      lptr pa = ks ? pa1 : pa0;
      lptr q0 = pb0 + ks * 4096, q1 = pb1 + ks * 4096;
      f.b[0] = trread2(q0, q0 + 512);
      f.a[0] = *(LDSAS bf16x8*)(pa);
      f.a[1] = *(LDSAS bf16x8*)(pa + 2048);
      f.a[2] = *(LDSAS bf16x8*)(pa + 4096);
      f.a[3] = *(LDSAS bf16x8*)(pa + 6144);
      f.b[1] = trread2(q1, q1 + 512);
      f.b[2] = trread2(q0 + 8192, q0 + 8192 + 512);
      f.b[3] = trread2(q1 + 8192, q1 + 8192 + 512);
      f.b[4] = trread2(q0 + 16384, q0 + 16384 + 512);
      f.b[5] = trread2(q1 + 16384, q1 + 16384 + 512);
    };
    auto mm1 = [&](const G1Frag& f, int i, int j) {
      acc[i][j] = i < 2 ? MFMA16(f.b[i], f.a[j], acc[i][j]) : MFMA16(f.a[j], f.b[i], acc[i][j]);
    };
    auto mm = [&](const G1Frag& f, int i0, int i1) {
#pragma unroll
      for (int i = i0; i < i1; ++i)
#pragma unroll
        for (int j = 0; j < 4; ++j) mm1(f, i, SNAKE && (i & 1) ? 3 - j : j);
    };
    auto ilv = [&]() {
#pragma unroll
      for (int i = 0; i < 16; ++i) {
        __builtin_amdgcn_sched_group_barrier(0x008, 1, 0);
        __builtin_amdgcn_sched_group_barrier(0x100, 1, 0);
      }
      sched0();
    };
    G1Frag F0, F1;
    u32x4 ones = {0x3f803f80u, 0x3f803f80u, 0x3f803f80u, 0x3f803f80u};
    lptr x1w = smem + L_SCR + wid * 1024 + lane * 16, x1r = smem + L_SCR + (wid ^ 2) * 1024 + lane * 16, x1zr = smem + X1_Z + (wid ^ 2) * 64 + g * 16;
    lptr x1zw = smem + X1_Z + wid * 64 + g * 16;
    const rsrc_t rp = mkrs(P.ws + WS_KVP, 1024 * KVP_SLOT);
    int vkvp = lane * 16 + (((hp * 2 + wh) * 2 + wr) * 16 + rb) * KVP_SLOT;
    int vzp = g * 16 + (l15 & 1) * 64 + 1024 + (((hp * 2 + wh) * 2 + wr) * 16 + rb) * KVP_SLOT;
    unsigned vpostP = (((lane & 7) * 4 + (hq & 3)) * 8 + ((lane >> 3) & 1) * 4 + (hq >> 2)) * MB_STRIDE;
    asm volatile("" : "+v"(ones), "+v"(x1w), "+v"(x1r), "+v"(x1zr), "+v"(x1zw), "+v"(vkvp), "+v"(vzp), "+v"(vpostP));
    __builtin_amdgcn_s_waitcnt(0xC07F);
    sched0();
    bar();
    STAMP(1);
    __builtin_amdgcn_s_waitcnt(0xC07F);
    ld(F0, 0);
    sched0();
    int ph = 0, delta = G1_STAGE;
    int beat_at = blockIdx.x == 0 ? (wid == 3 ? BEAT_AT : BEAT_AT - 1) : -1;
    asm volatile("" : "+s"(beat_at));
#pragma unroll 1
    for (int t = 0; t < 15; ++t) {
      ld(F1, 1); mm(F0, 0, 4); ilv();
      mm1(F0, 4, 0); sched0();
      mm1(F0, 4, 1); sched0();
      pa0 += delta; pb0 += delta; asm volatile("" : "+v"(pa0), "+v"(pb0)); sched0();
      mm1(F0, 4, 2); sched0();
      pa1 += delta; pb1 += delta; asm volatile("" : "+v"(pa1), "+v"(pb1)); sched0();
      mm1(F0, 4, 3); mm1(F0, 5, SNAKE ? 3 : 0); mm1(F0, 5, SNAKE ? 2 : 1); mm1(F0, 5, SNAKE ? 1 : 2);
      sched0();
      __builtin_amdgcn_s_waitcnt(0xC07F);
      mm1(F0, 5, SNAKE ? 0 : 3);
      sched0();
      bar();
      sched0();
      ld(F0, 0); mm(F1, 0, 4); ilv();
      mm1(F1, 4, 0); sched0();
      ph = ph == 2 ? 0 : ph + 1;
      asm volatile("" : "+s"(ph)); sched0();
      mm1(F1, 4, 1); sched0();
      delta = ph == 2 ? -2 * G1_STAGE : G1_STAGE;
      asm volatile("" : "+s"(delta)); sched0();
      mm1(F1, 4, 2); sched0();
      if (__builtin_expect(t == beat_at, 0)) {
        waitv<0>();
#pragma unroll 1
        for (int i = 0; i < (wid == 3 ? 4 : 0); ++i) __hip_atomic_fetch_add(hb_ptr(P.ws, i * 64 + lane), 1u, __ATOMIC_RELAXED, __HIP_MEMORY_SCOPE_AGENT);
      }
      sched0();
      mm1(F1, 4, 3);
      mm(F1, 5, 6);
      sched0();
    }
    __builtin_amdgcn_s_setprio(C_PRIO);
    {
      lptr q0 = pb0 + 4096, q1 = pb1 + 4096;
      F1.b[2] = trread2(q0 + 8192, q0 + 8192 + 512);
      F1.a[0] = *(LDSAS bf16x8*)(pa1);
      F1.a[1] = *(LDSAS bf16x8*)(pa1 + 2048);
      F1.a[2] = *(LDSAS bf16x8*)(pa1 + 4096);
      F1.a[3] = *(LDSAS bf16x8*)(pa1 + 6144);
      F1.b[3] = trread2(q1 + 8192, q1 + 8192 + 512);
      mm(F0, 2, 4);
#pragma unroll
      for (int i = 0; i < 8; ++i) {
        __builtin_amdgcn_sched_group_barrier(0x008, 1, 0);
        __builtin_amdgcn_sched_group_barrier(0x100, 1, 0);
      }
      sched0();
      F1.b[4] = trread2(q0 + 16384, q0 + 16384 + 512);
      F1.b[5] = trread2(q1 + 16384, q1 + 16384 + 512);
      F1.b[0] = trread2(q0, q0 + 512);
      F1.b[1] = trread2(q1, q1 + 512);
      mm(F1, 2, 4);
#pragma unroll
      for (int i = 0; i < 8; ++i) {
        __builtin_amdgcn_sched_group_barrier(0x008, 1, 0);
        __builtin_amdgcn_sched_group_barrier(0x100, 1, 0);
      }
      sched0();
      lset(smem, F_T15 + wid * 4, 1);
      sched0();
    }
    STAMP(2);
    u32x4 pkf[2][2], vf[2][2];
    f32x4 kvp[2][2], zz[2];
    const f32x4 zero4 = {0.f, 0.f, 0.f, 0.f};
    auto phik = [&](int u) {
#pragma unroll
      for (int c = 0; c < 2; ++c)
#pragma unroll
        for (int j = 0; j < 4; ++j) {
          const f32x4 kk = acc[2 + c][2 * u + (j >> 1)];
          pkf[u][c][j] = pack2(phi_f(kk[2 * (j & 1)]), phi_f(kk[2 * (j & 1) + 1]));
        }
    };
    auto packv = [&](int u) {
#pragma unroll
      for (int c = 0; c < 2; ++c)
#pragma unroll
        for (int j = 0; j < 4; ++j) {
          const f32x4 vv = acc[4 + c][2 * u + (j >> 1)];
          vf[u][c][j] = pack2(vv[2 * (j & 1)], vv[2 * (j & 1) + 1]);
        }
    };
    mm(F0, 4, 6);
    mm(F1, 4, 6);
    phik(0);
#pragma unroll
    for (int i = 0; i < 16; ++i) {
      __builtin_amdgcn_sched_group_barrier(0x008, 1, 0);
      __builtin_amdgcn_sched_group_barrier(0x402, 4, 0);
    }
    sched0();
    packv(0);
    sched0();
#pragma unroll
    for (int c = 0; c < 2; ++c) {
      kvp[c][1] = MFMA16(BC8(pkf[0][c]), BC8(vf[0][1]), zero4);
      kvp[c][0] = MFMA16(BC8(pkf[0][c]), BC8(vf[0][0]), zero4);
      zz[c] = MFMA16(BC8(pkf[0][c]), BC8(ones), zero4);
    }
    phik(1);
    packv(1);
#pragma unroll
    for (int i = 0; i < 6; ++i) {
      __builtin_amdgcn_sched_group_barrier(0x008, 1, 0);
      __builtin_amdgcn_sched_group_barrier(0x402, 5, 0);
    }
    sched0();
    kvp[0][1] = MFMA16(BC8(pkf[1][0]), BC8(vf[1][1]), kvp[0][1]);
    kvp[1][1] = MFMA16(BC8(pkf[1][1]), BC8(vf[1][1]), kvp[1][1]);
    zz[1] = MFMA16(BC8(pkf[1][1]), BC8(ones), zz[1]);
    sched0();
    kvp[0][0] = MFMA16(BC8(pkf[1][0]), BC8(vf[1][0]), kvp[0][0]);
    kvp[1][0] = MFMA16(BC8(pkf[1][1]), BC8(vf[1][0]), kvp[1][0]);
    zz[0] = MFMA16(BC8(pkf[1][0]), BC8(ones), zz[0]);
    sched0();
    {
      *(LDSAS u32x4*)(x1w) = u32x4{pack2(kvp[1][1][0], kvp[1][1][1]), pack2(kvp[1][1][2], kvp[1][1][3]), pack2(kvp[0][1][0], kvp[0][1][1]), pack2(kvp[0][1][2], kvp[0][1][3])};
      if (l15 == 0) *(LDSAS f32x4*)(x1zw) = zz[1];
      lset(smem, F_X1 + wid * 4, 1);
      u32x4 r;
      f32x4 rz;
      spin<0>([&]() {
        const int f = *(volatile LDSAS int*)(smem + F_X1 + (wid ^ 2) * 4);
        r = *(volatile LDSAS u32x4*)(x1r);
        rz = *(volatile LDSAS f32x4*)(x1zr);
        return __builtin_amdgcn_readfirstlane(f) != 0;
      });
      sched0();
      st_sc1(u32x4{pack2(kvp[0][0][0] + bflo(r[0]), kvp[0][0][1] + bfhi(r[0])), pack2(kvp[0][0][2] + bflo(r[1]), kvp[0][0][3] + bfhi(r[1])),
                   pack2(kvp[1][0][0] + bflo(r[2]), kvp[1][0][1] + bfhi(r[2])), pack2(kvp[1][0][2] + bflo(r[3]), kvp[1][0][3] + bfhi(r[3]))}, rp, vkvp, 0);
      const f32x4 zs = zz[0] + rz;
      asm volatile("s_mov_b64 exec, %3\n\tbuffer_store_dwordx4 %0, %1, %2, 0 offen sc1\n\ts_mov_b64 exec, -1\n\ts_nop 0"
                   ::"v"(zs), "v"(vzp), "s"(rp), "s"(0x0003000300030003ull) : "memory");
      {
        unsigned old;
        asm volatile("s_mov_b64 exec, 1\n\tds_add_rtn_u32 %0, %1, %2\n\ts_mov_b64 exec, -1\n\ts_waitcnt lgkmcnt(0)"
                     : "=&v"(old) : "v"((unsigned)(size_t)smem + F_NP), "v"(1u) : "memory");
        if (__builtin_amdgcn_readfirstlane(old) == 3) post<16>(cntb + MB_P * 4, vpostP);
      }
      sched0();
    }
    STAMP(3);
    auto phiq = [&](int r0, int r1) {
#pragma unroll
      for (int rt = r0; rt < r1; ++rt) {
#pragma unroll
        for (int j = 0; j < 8; ++j) pq[rt][j] = phi_f(acc[j >> 2][rt][j & 3]);
        pqf[rt] = u32x4{pack2(pq[rt][0], pq[rt][1]), pack2(pq[rt][2], pq[rt][3]), pack2(pq[rt][4], pq[rt][5]), pack2(pq[rt][6], pq[rt][7])};
        asm volatile("" : "+v"(pqf[rt]));
        sched0();
      }
    };
    const int headq = hq * 2 + wh;
    const char* zb = P.ws + WS_KVP + headq * 32 * KVP_SLOT + 1024;
    const char* pb = P.ws + WS_KVP + ((headq * 2 + wr) * 16 + 3) * KVP_SLOT;
    const char* pb2 = pb + 6 * KVP_SLOT;
    const char* pb3 = pb + 11 * KVP_SLOT;
    unsigned vz0 = g * 16 + (wr * 16 + l15) * KVP_SLOT, vz1 = g * 16 + ((wr ^ 1) * 16 + l15) * KVP_SLOT, vo = lane * 16;
    static_assert((16 - 2 * W1N) % 3 == 0, "");
    if (W1N < 8 && wid) wo_dma(P, smem, nt, voW, 2 * W1N + (wid - 1) * ((16 - 2 * W1N) / 3), (16 - 2 * W1N) / 3);
    if (wid == 0) poll_prime<0>(mbx + MB_P * 4);
    sched0();
    mm(F0, 0, 2);
    mm(F1, 0, 2);
    sched0();
    phiq(0, PQ_PRE);
    if (wid == 0) poll_prime<1>(mbx + MB_P * 4);
    u32x4 sel0, sel1;
    {
      const unsigned one = g == (l15 >> 2) ? ((l15 & 1) ? 0x3f800000u : 0x00003f80u) : 0u;
      const unsigned e0 = (l15 & 2) ? 0u : one, e1 = (l15 & 2) ? one : 0u;
      sel0 = u32x4{e0, e1, 0u, 0u};
      sel1 = u32x4{0u, 0u, e0, e1};
    }
    asm volatile("" : "+s"(zb), "+s"(pb), "+s"(pb2), "+s"(pb3), "+v"(vz0), "+v"(vz1), "+v"(vo), "+v"(sel0), "+v"(sel1));
    STAMP(4);
    group_wait<CSL, false>(smem, F_PR, wid == 0, mbx + MB_P * 4, 16);
    STAMP(5);
    auto pstage = [&](auto first_) {
      u32x4 z0, z1, pr[16];
      LDG(z0, vz0, zb, 0); LDG(z1, vz1, zb, 0);
      sfor<0, 16>([&](auto p_) {
        constexpr int p = decltype(p_)::v;
        ldg_sc1<(p - (p < 6 ? 3 : p < 12 ? 9 : 14)) * KVP_SLOT>(pr[p], vo, p < 6 ? pb : p < 12 ? pb2 : pb3);
      });
      sched0();
      if constexpr (decltype(first_)::v) phiq(PQ_PRE, 4);
      asm volatile("s_waitcnt vmcnt(16)" : "+v"(z0), "+v"(z1)::"memory");
      float z[8], rd[4];
#pragma unroll
      for (int j = 0; j < 4; ++j) { z[j] = u2f(z0[j]); z[4 + j] = u2f(z1[j]); }
#pragma unroll
      for (int j = 0; j < 8; ++j) z[j] += row_ror<8>(z[j]);
#pragma unroll
      for (int j = 0; j < 8; ++j) z[j] += row_ror<4>(z[j]);
#pragma unroll
      for (int j = 0; j < 8; ++j) z[j] += row_ror<2>(z[j]);
#pragma unroll
      for (int j = 0; j < 8; ++j) z[j] += row_ror<1>(z[j]);
      const u32x4 zf = {pack2(z[0], z[1]), pack2(z[2], z[3]), pack2(z[4], z[5]), pack2(z[6], z[7])};
#pragma unroll
      for (int rt = 0; rt < 4; ++rt) {
        const f32x4 den = MFMA16(BC8(zf), BC8(pqf[rt]), zero4);
        rd[rt] = __builtin_amdgcn_rcpf(den[0] + 1e-6f);
      }
      s0 = zero4; s1 = zero4;
      sfor<0, 4>([&](auto i_) {
        constexpr int i = decltype(i_)::v;
        sched0();
        asm volatile("s_waitcnt vmcnt(%4)" : "+v"(pr[4 * i]), "+v"(pr[4 * i + 1]), "+v"(pr[4 * i + 2]), "+v"(pr[4 * i + 3]) : "i"(12 - 4 * i) : "memory");
        if constexpr (i == 3) { if (wid == 0) poll_prime<0>(mbx + MB_O * 4); }
#pragma unroll
        for (int p = 4 * i; p < 4 * i + 4; ++p) { s0 = MFMA16(BC8(sel0), BC8(pr[p]), s0); s1 = MFMA16(BC8(sel1), BC8(pr[p]), s1); }
        const float r = rd[i];
        pqs[i] = u32x4{pack2(pq[i][0] * r, pq[i][1] * r), pack2(pq[i][2] * r, pq[i][3] * r), pack2(pq[i][4] * r, pq[i][5] * r), pack2(pq[i][6] * r, pq[i][7] * r)};
        asm volatile("" : "+v"(pqs[i]));
#pragma unroll
        for (int j = 0; j < 8; ++j) {
          __builtin_amdgcn_sched_group_barrier(0x008, 1, 0);
          __builtin_amdgcn_sched_group_barrier(0x002, 2, 0);
        }
        sched0();
      });
      whv = *(volatile LDSAS u32x4*)(smem + F_HR);
      if (__builtin_expect(!any_lane(s0[0] != s0[0] || rd[0] != rd[0]), 1)) return true;
      bool m = has_mark(z0) | has_mark(z1);
#pragma unroll
      for (int p = 0; p < 16; ++p) m |= has_mark(pr[p]);
      return !any_lane(m);
    };
    if (__builtin_expect(!pstage(IC<1>{}), 0)) {
#pragma unroll 1
      for (int att = 0; att < SPIN_LIMIT; ++att) {
        if (lane == 0) CNT_SLOW(0);
        if (pstage(IC<0>{})) break;
      }
    }
  }
  {
    const f32x4 zero4 = {0.f, 0.f, 0.f, 0.f};
    STAMP(6);
    *(LDSAS u32x4*)(xp) = u32x4{pack2(s0[0], s0[1]), pack2(s0[2], s0[3]), pack2(s1[0], s1[1]), pack2(s1[2], s1[3])};
    lset(smem, F_X2 + wid * 4, 1);
    if (__builtin_amdgcn_readfirstlane(whv[0] & whv[1] & whv[2] & whv[3]) == 0) spin<0>([&]() { return lmin4(smem, F_HR) != 0; });
    u32x2 k0a, k0b, k1a, k1b;
    spin<0>([&]() {
      const int f = *(volatile LDSAS int*)(smem + F_X2 + (wid ^ 2) * 4);
      k0a = *(volatile LDSAS u32x2*)(k0); k0b = *(volatile LDSAS u32x2*)(k0x); k1a = *(volatile LDSAS u32x2*)(k1); k1b = *(volatile LDSAS u32x2*)(k1x);
      return __builtin_amdgcn_readfirstlane(f) != 0;
    });
    sched0();
    const u32x4 kv0 = {k0a[0], k0a[1], k0b[0], k0b[1]}, kv1 = {k1a[0], k1a[1], k1b[0], k1b[1]};
    f32x4 n0[4], n1[4];
#pragma unroll
    for (int rt = 0; rt < 4; ++rt) {
      n0[rt] = MFMA16(BC8(kv0), BC8(pqs[rt]), zero4);
      n1[rt] = MFMA16(BC8(kv1), BC8(pqs[rt]), zero4);
    }
    sched0();
#pragma unroll
    for (int rt = 0; rt < 4; ++rt) {
      const u32x4 o = {pack2(n0[rt][0], n0[rt][1]), pack2(n0[rt][2], n0[rt][3]), pack2(n1[rt][0], n1[rt][1]), pack2(n1[rt][2], n1[rt][3])};
      st_sc1(o, ro, (rt >> 1) ? vst2 : vst, (rt & 1) * 1024);
      sched0();
    }
    if (wid == 0) { poll_prime<1>(mbx + MB_O * 4); waitv<1>(); } else waitv<0>();
    {
      unsigned old;
      asm volatile("s_mov_b64 exec, 1\n\tds_add_rtn_u32 %0, %1, %2\n\ts_mov_b64 exec, -1\n\ts_waitcnt lgkmcnt(0)"
                   : "=&v"(old) : "v"((unsigned)(size_t)smem + F_NO), "v"(1u) : "memory");
      if (wid == 0) {
        unsigned t;
        asm volatile("v_readfirstlane_b32 %0, v253" : "=s"(t)::"memory");
        oready = __builtin_amdgcn_readfirstlane(t) >= 12u;
        if (oready) lset(smem, F_OR, 1);
      }
      if (__builtin_amdgcn_readfirstlane(old) == 3) post<16>(cntb + ((hq & 6) == 2 ? MB_O2 : MB_O) * 4, vpostO);
    }
    STAMP(7);
  }
  {
    const int rb2 = G2MAP ? 2 * xl + (idx >> 4) : 4 * (xl >> 1) + (idx >> 3);
    const int wb = (4 * g + q4) * 128 + (p4 & 1) * 8 + (((p4 >> 1) ^ ((((g & 1) << 1) | (q4 >> 1)) << 1)) << 4);
    const char* ob = P.ws + WS_KVT + rb2 * 262144 + wid * 4096;
    unsigned vo = lane * 16;
    asm volatile("" : "+v"(vo));
    spin<0>([&]() { return lmin4(smem, F_X2) != 0; });
    if (wid) wo_dma(P, smem, nt, voW, 11 + wid * 5 + W3_EARLY, (wid == 3 ? 4 : 5) - W3_EARLY);
    f32x4 acc[4][2];
    {
      u32x4 of[OD];
      lptr w[4];
#pragma unroll
      for (int T = 0; T < 4; ++T) { w[T] = smem + (wb ^ (T << 5)); asm volatile("" : "+v"(w[T])); }
      bf16x8 a[3][4];
      u32x4 fl;
#pragma unroll
      for (int h = 0; h < 2; ++h)
#pragma unroll
        for (int T = 0; T < 4; ++T) { a[h][T] = trread2(w[T] + HD(h) * 4096, w[T] + HD(h) * 4096 + 2048); sched0(); }
#pragma unroll
      for (int i = 0; i < 4; ++i) { acc[i][0] = f32x4{0.f, 0.f, 0.f, 0.f}; acc[i][1] = acc[i][0]; asm volatile("" : "+v"(acc[i][0]), "+v"(acc[i][1])); }
      if (!oready) group_wait<CSL, false>(smem, F_OR, wid == 0, mbx + MB_O * 4, 12);
      __builtin_amdgcn_s_setprio(G2_PRIO);
      STAMP(8);
      auto ldo = [&](auto f_) {
        constexpr int f = decltype(f_)::v;
        ldg_sc1<(f & 3) * 1024>(of[f % OD], vo, ob + (AH(f >> 1) >> 1) * 16384);
      };
      auto check = [&](auto h_, auto n_) {
        constexpr int h = decltype(h_)::v;
        u32x4 &f0 = of[(2 * h) % OD], &f1 = of[(2 * h + 1) % OD];
        asm volatile("s_waitcnt vmcnt(%2)" : "+v"(f0), "+v"(f1) : "i"(decltype(n_)::v) : "memory");
      };
      sfor<0, OD>(ldo);
      check(IC<0>{}, IC<OD - 2>{});
      sfor<0, 32>([&](auto h_) {
        constexpr int h = decltype(h_)::v;
        constexpr int issued = 2 * h + OD < 64 ? 2 * h + OD : 64;
        __builtin_amdgcn_s_waitcnt(h == 31 ? 0xC07F : 0xC87F);
        sched0();
        if constexpr (h < 30 && (HD(h + 2) >> 4) != (HD(h + 1) >> 4)) {
#pragma unroll
          for (int T = 0; T < 4; ++T) { w[T] += HD(h + 2) >= 16 ? 65536 : -65536; asm volatile("" : "+v"(w[T])); }
        }
        if constexpr (h == 0) { lset(smem, F_WC + wid * 4, 1); sched0(); }
        if constexpr (h == 14 || h == 28) {
          if (__builtin_expect(__builtin_amdgcn_readfirstlane(fl[0] & fl[1] & fl[2] & fl[3]) == 0, 0))
            spin<1>([&]() { return lmin4(smem, h == 14 ? F_WC : F_WL) != 0; });
          sched0();
        }
        if constexpr (h == 13 || h == 27) { fl = *(volatile LDSAS u32x4*)(smem + (h == 13 ? F_WC : F_WL)); sched0(); }
        constexpr int o = (HD(h < 30 ? h + 2 : 0) & 15) * 4096;
        auto half = [&](int T0) {
#pragma unroll
          for (int T = T0; T < T0 + 2; ++T) {
            if constexpr (h < 30) a[(h + 2) % 3][T] = trread2(w[T] + o, w[T] + o + 2048);
            acc[T][0] = MFMA16(a[h % 3][T], BC8(of[(2 * h) % OD]), acc[T][0]);
            acc[T][1] = MFMA16(a[h % 3][T], BC8(of[(2 * h + 1) % OD]), acc[T][1]);
          }
#pragma unroll
          for (int j = 0; j < 4; ++j) {
            __builtin_amdgcn_sched_group_barrier(0x008, 1, 0);
            __builtin_amdgcn_sched_group_barrier(0x100, 1, 0);
          }
          sched0();
        };
        half(0);
        constexpr int LS = 24 - OD / 2;
        static_assert(O2_AT < LS && LS <= O2_AT + OD / 2 - 1, "");
        if constexpr (h < 31) check(IC<h + 1>{}, IC<issued - 2 * h - 4 + (h > O2_AT && h <= LS)>{});
        sched0();
        half(2);
        if constexpr (h == LS) {
          unsigned t;
          asm volatile("s_waitcnt vmcnt(%1)\n\tv_readfirstlane_b32 %0, v253" : "=s"(t) : "i"(2 * (LS - 1 - O2_AT)) : "memory");
          if (__builtin_expect(__builtin_amdgcn_readfirstlane(t) < 4u, 0)) { if (lane == 0) CNT_SLOW(3); group_wait<0, true>(smem, F_OR, true, mbx + MB_O2 * 4, 4); }
          sched0();
        }
        if constexpr (2 * h + OD < 64) { ldo(IC<2 * h + OD>{}); ldo(IC<2 * h + OD + 1>{}); }
        if constexpr (h == O2_AT) poll_prime<0>(mbx + MB_O2 * 4);
        sched0();
      });
    }
    STAMP(9);
#pragma unroll
    for (int st = 0; st < 2; ++st) {
      const unsigned dst = ((rb2 * 128 + wid * 32 + st * 16 + l15) * 2048 + 1024 + nt * 64 + 16 * (g & 1) + 8 * (g >> 1)) * 2;
#pragma unroll
      for (int tp = 0; tp < 2; ++tp) {
        const f32x4 u = acc[2 * tp][st], v = acc[2 * tp + 1][st];
        auto r0 = __builtin_amdgcn_permlane16_swap(pack2(u[0], u[1]), pack2(v[0], v[1]), false, false);
        auto r1 = __builtin_amdgcn_permlane16_swap(pack2(u[2], u[3]), pack2(v[2], v[3]), false, false);
        const u32x4 o = {r0[0], r1[0], r0[1], r1[1]};
        st_out<OUTAUX>(o, P.out, dst + 64 * tp);
      }
    }
  }
  STAMP(10);
  ST_OUT;
  PAD_END;
}

#define CONV_ASM \
  "s_mov_b32 vcc_lo, 0\n\ts_mov_b32 vcc_hi, 0x80000000\n\ts_mov_b64 s[40:41], %14\n\t" \
  "s_mov_b32 s42, %15\n\tds_read_b64_tr_b16 v[34:35], %8 offset:0\n\tds_read_b64_tr_b16 v[36:37], %8 offset:32\n\t" \
  "ds_read_b64_tr_b16 v[98:99], %4 offset:0\n\tds_read_b64_tr_b16 v[100:101], %5 offset:0\n\tds_read_b64_tr_b16 v[102:103], %6 offset:0\n\t" \
  "ds_read_b64_tr_b16 v[104:105], %7 offset:0\n\tds_read_b64_tr_b16 v[38:39], %8 offset:256\n\tds_read_b64_tr_b16 v[40:41], %8 offset:288\n\t" \
  "ds_read_b64_tr_b16 v[106:107], %4 offset:1024\n\tds_read_b64_tr_b16 v[108:109], %5 offset:1024\n\tds_read_b64_tr_b16 v[110:111], %6 offset:1024\n\t" \
  "ds_read_b64_tr_b16 v[112:113], %7 offset:1024\n\tds_read_b64_tr_b16 v[42:43], %8 offset:512\n\tds_read_b64_tr_b16 v[44:45], %8 offset:544\n\t" \
  "ds_read_b64_tr_b16 v[114:115], %4 offset:2048\n\tds_read_b64_tr_b16 v[116:117], %5 offset:2048\n\tds_read_b64_tr_b16 v[118:119], %6 offset:2048\n\t" \
  "ds_read_b64_tr_b16 v[120:121], %7 offset:2048\n\tv_mov_b32 v247, 0\n\tv_mov_b32 v248, 0\n\t" \
  "v_mov_b32 v249, 0\n\ts_waitcnt lgkmcnt(14)\n\tv_mfma_f32_16x16x32_bf16 v[226:229], v[98:101], v[34:37], 0\n\t" \
  "ds_read_b64_tr_b16 v[46:47], %8 offset:768\n\tds_read_b64_tr_b16 v[48:49], %8 offset:800\n\tds_read_b64_tr_b16 v[122:123], %4 offset:3072\n\t" \
  "ds_read_b64_tr_b16 v[124:125], %5 offset:3072\n\tds_read_b64_tr_b16 v[126:127], %6 offset:3072\n\tds_read_b64_tr_b16 v[128:129], %7 offset:3072\n\t" \
  "s_waitcnt lgkmcnt(15)\n\tv_mfma_f32_16x16x32_bf16 v[230:233], v[102:105], v[34:37], 0\n\ts_waitcnt lgkmcnt(14)\n\t" \
  "v_mfma_f32_16x16x32_bf16 v[234:237], v[106:109], v[34:37], 0\n\tds_read_b64_tr_b16 v[50:51], %8 offset:1024\n\tds_read_b64_tr_b16 v[52:53], %8 offset:1056\n\t" \
  "ds_read_b64_tr_b16 v[130:131], %4 offset:4096\n\tds_read_b64_tr_b16 v[132:133], %5 offset:4096\n\tds_read_b64_tr_b16 v[134:135], %6 offset:4096\n\t" \
  "ds_read_b64_tr_b16 v[136:137], %7 offset:4096\n\ts_waitcnt lgkmcnt(15)\n\tv_mfma_f32_16x16x32_bf16 v[238:241], v[110:113], v[34:37], 0\n\t" \
  "v_cndmask_b32_e32 v250, v229, v249, vcc\n\tv_cndmask_b32_e32 v251, v233, v229, vcc\n\ts_nop 0\n\t" \
  "v_add_f32_dpp v228, v250, v228 wave_ror:1 row_mask:0xf bank_mask:0xf bound_ctrl:1\n\tv_add_f32_dpp v232, v251, v232 wave_ror:1 row_mask:0xf bank_mask:0xf bound_ctrl:1\n\tv_mfma_f32_16x16x32_bf16 v[234:237], v[98:101], v[38:41], v[234:237]\n\t" \
  "v_cndmask_b32_e32 v250, v228, v248, vcc\n\tv_cndmask_b32_e32 v251, v232, v228, vcc\n\ts_nop 0\n\t" \
  "v_add_f32_dpp v227, v250, v227 wave_ror:1 row_mask:0xf bank_mask:0xf bound_ctrl:1\n\tv_add_f32_dpp v231, v251, v231 wave_ror:1 row_mask:0xf bank_mask:0xf bound_ctrl:1\n\tv_cndmask_b32_e32 v250, v227, v247, vcc\n\t" \
  "v_cndmask_b32_e32 v251, v231, v227, vcc\n\tv_mfma_f32_16x16x32_bf16 v[238:241], v[102:105], v[38:41], v[238:241]\n\tv_add_f32_dpp v226, v250, v226 wave_ror:1 row_mask:0xf bank_mask:0xf bound_ctrl:1\n\t" \
  "v_add_f32_dpp v230, v251, v230 wave_ror:1 row_mask:0xf bank_mask:0xf bound_ctrl:1\n\tv_cvt_pk_bf16_f32 v252, v226, v230\n\tv_mfma_f32_16x16x32_bf16 v[242:245], v[106:109], v[38:41], 0\n\t" \
  "ds_read_b64_tr_b16 v[54:55], %8 offset:1280\n\tds_read_b64_tr_b16 v[56:57], %8 offset:1312\n\tds_read_b64_tr_b16 v[138:139], %4 offset:5120\n\t" \
  "ds_read_b64_tr_b16 v[140:141], %5 offset:5120\n\tv_mfma_f32_16x16x32_bf16 v[246:249], v[110:113], v[38:41], 0\n\tds_read_b64_tr_b16 v[142:143], %6 offset:5120\n\t" \
  "ds_read_b64_tr_b16 v[144:145], %7 offset:5120\n\tv_cndmask_b32_e32 v250, v237, v233, vcc\n\tv_cndmask_b32_e32 v251, v241, v237, vcc\n\t" \
  "s_waitcnt lgkmcnt(15)\n\tv_mfma_f32_16x16x32_bf16 v[242:245], v[114:117], v[34:37], v[242:245]\n\tv_add_f32_dpp v236, v250, v236 wave_ror:1 row_mask:0xf bank_mask:0xf bound_ctrl:1\n\t" \
  "v_add_f32_dpp v240, v251, v240 wave_ror:1 row_mask:0xf bank_mask:0xf bound_ctrl:1\n\tv_mfma_f32_16x16x32_bf16 v[246:249], v[118:121], v[34:37], v[246:249]\n\tv_cndmask_b32_e32 v250, v236, v232, vcc\n\t" \
  "v_cndmask_b32_e32 v251, v240, v236, vcc\n\ts_nop 0\n\tv_add_f32_dpp v235, v250, v235 wave_ror:1 row_mask:0xf bank_mask:0xf bound_ctrl:1\n\t" \
  "v_add_f32_dpp v239, v251, v239 wave_ror:1 row_mask:0xf bank_mask:0xf bound_ctrl:1\n\tv_mfma_f32_16x16x32_bf16 v[242:245], v[98:101], v[42:45], v[242:245]\n\tv_cndmask_b32_e32 v250, v235, v231, vcc\n\t" \
  "v_cndmask_b32_e32 v251, v239, v235, vcc\n\ts_nop 0\n\tv_add_f32_dpp v234, v250, v234 wave_ror:1 row_mask:0xf bank_mask:0xf bound_ctrl:1\n\t" \
  "v_add_f32_dpp v238, v251, v238 wave_ror:1 row_mask:0xf bank_mask:0xf bound_ctrl:1\n\tv_mfma_f32_16x16x32_bf16 v[246:249], v[102:105], v[42:45], v[246:249]\n\tv_cvt_pk_bf16_f32 v253, v234, v238\n\t" \
  "v_mfma_f32_16x16x32_bf16 v[226:229], v[114:117], v[38:41], 0\n\tds_read_b64_tr_b16 v[58:59], %8 offset:1536\n\tds_read_b64_tr_b16 v[60:61], %8 offset:1568\n\t" \
  "ds_read_b64_tr_b16 v[146:147], %4 offset:6144\n\tds_read_b64_tr_b16 v[148:149], %5 offset:6144\n\tv_mfma_f32_16x16x32_bf16 v[230:233], v[118:121], v[38:41], 0\n\t" \
  "ds_read_b64_tr_b16 v[150:151], %6 offset:6144\n\tds_read_b64_tr_b16 v[152:153], %7 offset:6144\n\tv_mfma_f32_16x16x32_bf16 v[226:229], v[106:109], v[42:45], v[226:229]\n\t" \
  "v_cndmask_b32_e32 v250, v245, v241, vcc\n\tv_cndmask_b32_e32 v251, v249, v245, vcc\n\tv_mfma_f32_16x16x32_bf16 v[230:233], v[110:113], v[42:45], v[230:233]\n\t" \
  "v_add_f32_dpp v244, v250, v244 wave_ror:1 row_mask:0xf bank_mask:0xf bound_ctrl:1\n\tv_add_f32_dpp v248, v251, v248 wave_ror:1 row_mask:0xf bank_mask:0xf bound_ctrl:1\n\ts_waitcnt lgkmcnt(15)\n\t" \
  "v_mfma_f32_16x16x32_bf16 v[226:229], v[122:125], v[34:37], v[226:229]\n\tv_cndmask_b32_e32 v250, v244, v240, vcc\n\tv_cndmask_b32_e32 v251, v248, v244, vcc\n\t" \
  "s_nop 0\n\tv_add_f32_dpp v243, v250, v243 wave_ror:1 row_mask:0xf bank_mask:0xf bound_ctrl:1\n\tv_add_f32_dpp v247, v251, v247 wave_ror:1 row_mask:0xf bank_mask:0xf bound_ctrl:1\n\t" \
  "v_mfma_f32_16x16x32_bf16 v[230:233], v[126:129], v[34:37], v[230:233]\n\tv_cndmask_b32_e32 v250, v243, v239, vcc\n\tv_cndmask_b32_e32 v251, v247, v243, vcc\n\t" \
  "v_mfma_f32_16x16x32_bf16 v[226:229], v[98:101], v[46:49], v[226:229]\n\tv_add_f32_dpp v242, v250, v242 wave_ror:1 row_mask:0xf bank_mask:0xf bound_ctrl:1\n\tv_add_f32_dpp v246, v251, v246 wave_ror:1 row_mask:0xf bank_mask:0xf bound_ctrl:1\n\t" \
  "v_mfma_f32_16x16x32_bf16 v[230:233], v[102:105], v[46:49], v[230:233]\n\tv_cvt_pk_bf16_f32 v254, v242, v246\n\tv_mfma_f32_16x16x32_bf16 v[234:237], v[122:125], v[38:41], 0\n\t" \
  "ds_read_b64_tr_b16 v[62:63], %8 offset:1792\n\tds_read_b64_tr_b16 v[64:65], %8 offset:1824\n\tv_mfma_f32_16x16x32_bf16 v[238:241], v[126:129], v[38:41], 0\n\t" \
  "ds_read_b64_tr_b16 v[154:155], %4 offset:7168\n\tds_read_b64_tr_b16 v[156:157], %5 offset:7168\n\tv_mfma_f32_16x16x32_bf16 v[234:237], v[114:117], v[42:45], v[234:237]\n\t" \
  "ds_read_b64_tr_b16 v[158:159], %6 offset:7168\n\tds_read_b64_tr_b16 v[160:161], %7 offset:7168\n\tv_mfma_f32_16x16x32_bf16 v[238:241], v[118:121], v[42:45], v[238:241]\n\t" \
  "v_cndmask_b32_e32 v250, v229, v249, vcc\n\tv_cndmask_b32_e32 v251, v233, v229, vcc\n\tv_mfma_f32_16x16x32_bf16 v[234:237], v[106:109], v[46:49], v[234:237]\n\t" \
  "v_add_f32_dpp v228, v250, v228 wave_ror:1 row_mask:0xf bank_mask:0xf bound_ctrl:1\n\tv_add_f32_dpp v232, v251, v232 wave_ror:1 row_mask:0xf bank_mask:0xf bound_ctrl:1\n\tv_mfma_f32_16x16x32_bf16 v[238:241], v[110:113], v[46:49], v[238:241]\n\t" \
  "v_cndmask_b32_e32 v250, v228, v248, vcc\n\tv_cndmask_b32_e32 v251, v232, v228, vcc\n\ts_waitcnt lgkmcnt(15)\n\t" \
  "v_mfma_f32_16x16x32_bf16 v[234:237], v[130:133], v[34:37], v[234:237]\n\tv_add_f32_dpp v227, v250, v227 wave_ror:1 row_mask:0xf bank_mask:0xf bound_ctrl:1\n\tv_add_f32_dpp v231, v251, v231 wave_ror:1 row_mask:0xf bank_mask:0xf bound_ctrl:1\n\t" \
  "v_mfma_f32_16x16x32_bf16 v[238:241], v[134:137], v[34:37], v[238:241]\n\tv_cndmask_b32_e32 v250, v227, v247, vcc\n\tv_cndmask_b32_e32 v251, v231, v227, vcc\n\t" \
  "v_mfma_f32_16x16x32_bf16 v[234:237], v[98:101], v[50:53], v[234:237]\n\tv_add_f32_dpp v226, v250, v226 wave_ror:1 row_mask:0xf bank_mask:0xf bound_ctrl:1\n\tv_add_f32_dpp v230, v251, v230 wave_ror:1 row_mask:0xf bank_mask:0xf bound_ctrl:1\n\t" \
  "v_mfma_f32_16x16x32_bf16 v[238:241], v[102:105], v[50:53], v[238:241]\n\tv_cvt_pk_bf16_f32 v255, v226, v230\n\tbuffer_store_dwordx4 v[252:255], %9, %13, 0 offen offset:0 sc1\n\t" \
  "v_mfma_f32_16x16x32_bf16 v[242:245], v[130:133], v[38:41], 0\n\tds_read_b64_tr_b16 v[66:67], %8 offset:2048\n\tds_read_b64_tr_b16 v[68:69], %8 offset:2080\n\t" \
  "v_mfma_f32_16x16x32_bf16 v[246:249], v[134:137], v[38:41], 0\n\tds_read_b64_tr_b16 v[162:163], %4 offset:8192\n\tds_read_b64_tr_b16 v[164:165], %5 offset:8192\n\t" \
  "v_mfma_f32_16x16x32_bf16 v[242:245], v[122:125], v[42:45], v[242:245]\n\tds_read_b64_tr_b16 v[166:167], %6 offset:8192\n\tds_read_b64_tr_b16 v[168:169], %7 offset:8192\n\t" \
  "v_mfma_f32_16x16x32_bf16 v[246:249], v[126:129], v[42:45], v[246:249]\n\tv_cndmask_b32_e32 v250, v237, v233, vcc\n\tv_cndmask_b32_e32 v251, v241, v237, vcc\n\t" \
  "v_mfma_f32_16x16x32_bf16 v[242:245], v[114:117], v[46:49], v[242:245]\n\tv_add_f32_dpp v236, v250, v236 wave_ror:1 row_mask:0xf bank_mask:0xf bound_ctrl:1\n\tv_add_f32_dpp v240, v251, v240 wave_ror:1 row_mask:0xf bank_mask:0xf bound_ctrl:1\n\t" \
  "v_mfma_f32_16x16x32_bf16 v[246:249], v[118:121], v[46:49], v[246:249]\n\tv_cndmask_b32_e32 v250, v236, v232, vcc\n\tv_cndmask_b32_e32 v251, v240, v236, vcc\n\t" \
  "v_mfma_f32_16x16x32_bf16 v[242:245], v[106:109], v[50:53], v[242:245]\n\tv_add_f32_dpp v235, v250, v235 wave_ror:1 row_mask:0xf bank_mask:0xf bound_ctrl:1\n\tv_add_f32_dpp v239, v251, v239 wave_ror:1 row_mask:0xf bank_mask:0xf bound_ctrl:1\n\t" \
  "v_mfma_f32_16x16x32_bf16 v[246:249], v[110:113], v[50:53], v[246:249]\n\ts_waitcnt lgkmcnt(15)\n\tv_mfma_f32_16x16x32_bf16 v[242:245], v[138:141], v[34:37], v[242:245]\n\t" \
  "v_cndmask_b32_e32 v250, v235, v231, vcc\n\tv_cndmask_b32_e32 v251, v239, v235, vcc\n\tv_mfma_f32_16x16x32_bf16 v[246:249], v[142:145], v[34:37], v[246:249]\n\t" \
  "v_add_f32_dpp v234, v250, v234 wave_ror:1 row_mask:0xf bank_mask:0xf bound_ctrl:1\n\tv_add_f32_dpp v238, v251, v238 wave_ror:1 row_mask:0xf bank_mask:0xf bound_ctrl:1\n\tv_mfma_f32_16x16x32_bf16 v[242:245], v[98:101], v[54:57], v[242:245]\n\t" \
  "v_cvt_pk_bf16_f32 v252, v234, v238\n\tv_mfma_f32_16x16x32_bf16 v[246:249], v[102:105], v[54:57], v[246:249]\n\tv_mfma_f32_16x16x32_bf16 v[226:229], v[138:141], v[38:41], 0\n\t" \
  "ds_read_b64_tr_b16 v[70:71], %8 offset:2304\n\tds_read_b64_tr_b16 v[72:73], %8 offset:2336\n\tv_mfma_f32_16x16x32_bf16 v[230:233], v[142:145], v[38:41], 0\n\t" \
  "ds_read_b64_tr_b16 v[170:171], %4 offset:9216\n\tds_read_b64_tr_b16 v[172:173], %5 offset:9216\n\tv_mfma_f32_16x16x32_bf16 v[226:229], v[130:133], v[42:45], v[226:229]\n\t" \
  "ds_read_b64_tr_b16 v[174:175], %6 offset:9216\n\tds_read_b64_tr_b16 v[176:177], %7 offset:9216\n\tv_mfma_f32_16x16x32_bf16 v[230:233], v[134:137], v[42:45], v[230:233]\n\t" \
  "v_cndmask_b32_e32 v250, v245, v241, vcc\n\tv_cndmask_b32_e32 v251, v249, v245, vcc\n\tv_mfma_f32_16x16x32_bf16 v[226:229], v[122:125], v[46:49], v[226:229]\n\t" \
  "v_add_f32_dpp v244, v250, v244 wave_ror:1 row_mask:0xf bank_mask:0xf bound_ctrl:1\n\tv_add_f32_dpp v248, v251, v248 wave_ror:1 row_mask:0xf bank_mask:0xf bound_ctrl:1\n\tv_mfma_f32_16x16x32_bf16 v[230:233], v[126:129], v[46:49], v[230:233]\n\t" \
  "v_mfma_f32_16x16x32_bf16 v[226:229], v[114:117], v[50:53], v[226:229]\n\tv_cndmask_b32_e32 v250, v244, v240, vcc\n\tv_cndmask_b32_e32 v251, v248, v244, vcc\n\t" \
  "v_mfma_f32_16x16x32_bf16 v[230:233], v[118:121], v[50:53], v[230:233]\n\tv_add_f32_dpp v243, v250, v243 wave_ror:1 row_mask:0xf bank_mask:0xf bound_ctrl:1\n\tv_add_f32_dpp v247, v251, v247 wave_ror:1 row_mask:0xf bank_mask:0xf bound_ctrl:1\n\t" \
  "v_mfma_f32_16x16x32_bf16 v[226:229], v[106:109], v[54:57], v[226:229]\n\tv_mfma_f32_16x16x32_bf16 v[230:233], v[110:113], v[54:57], v[230:233]\n\tv_cndmask_b32_e32 v250, v243, v239, vcc\n\t" \
  "v_cndmask_b32_e32 v251, v247, v243, vcc\n\ts_waitcnt lgkmcnt(15)\n\tv_mfma_f32_16x16x32_bf16 v[226:229], v[146:149], v[34:37], v[226:229]\n\t" \
  "v_add_f32_dpp v242, v250, v242 wave_ror:1 row_mask:0xf bank_mask:0xf bound_ctrl:1\n\tv_add_f32_dpp v246, v251, v246 wave_ror:1 row_mask:0xf bank_mask:0xf bound_ctrl:1\n\tv_mfma_f32_16x16x32_bf16 v[230:233], v[150:153], v[34:37], v[230:233]\n\t" \
  "v_mfma_f32_16x16x32_bf16 v[226:229], v[98:101], v[58:61], v[226:229]\n\tv_cvt_pk_bf16_f32 v253, v242, v246\n\tv_mfma_f32_16x16x32_bf16 v[230:233], v[102:105], v[58:61], v[230:233]\n\t" \
  "v_mfma_f32_16x16x32_bf16 v[234:237], v[146:149], v[38:41], 0\n\tds_read_b64_tr_b16 v[74:75], %8 offset:2560\n\tds_read_b64_tr_b16 v[76:77], %8 offset:2592\n\t" \
  "v_mfma_f32_16x16x32_bf16 v[238:241], v[150:153], v[38:41], 0\n\tds_read_b64_tr_b16 v[178:179], %4 offset:10240\n\tds_read_b64_tr_b16 v[180:181], %5 offset:10240\n\t" \
  "v_mfma_f32_16x16x32_bf16 v[234:237], v[138:141], v[42:45], v[234:237]\n\tds_read_b64_tr_b16 v[182:183], %6 offset:10240\n\tds_read_b64_tr_b16 v[184:185], %7 offset:10240\n\t" \
  "v_mfma_f32_16x16x32_bf16 v[238:241], v[142:145], v[42:45], v[238:241]\n\tv_cndmask_b32_e32 v250, v229, v249, vcc\n\tv_cndmask_b32_e32 v251, v233, v229, vcc\n\t" \
  "v_mfma_f32_16x16x32_bf16 v[234:237], v[130:133], v[46:49], v[234:237]\n\tv_add_f32_dpp v228, v250, v228 wave_ror:1 row_mask:0xf bank_mask:0xf bound_ctrl:1\n\tv_add_f32_dpp v232, v251, v232 wave_ror:1 row_mask:0xf bank_mask:0xf bound_ctrl:1\n\t" \
  "v_mfma_f32_16x16x32_bf16 v[238:241], v[134:137], v[46:49], v[238:241]\n\tv_mfma_f32_16x16x32_bf16 v[234:237], v[122:125], v[50:53], v[234:237]\n\tv_cndmask_b32_e32 v250, v228, v248, vcc\n\t" \
  "v_cndmask_b32_e32 v251, v232, v228, vcc\n\tv_mfma_f32_16x16x32_bf16 v[238:241], v[126:129], v[50:53], v[238:241]\n\tv_mfma_f32_16x16x32_bf16 v[234:237], v[114:117], v[54:57], v[234:237]\n\t" \
  "v_add_f32_dpp v227, v250, v227 wave_ror:1 row_mask:0xf bank_mask:0xf bound_ctrl:1\n\tv_add_f32_dpp v231, v251, v231 wave_ror:1 row_mask:0xf bank_mask:0xf bound_ctrl:1\n\tv_mfma_f32_16x16x32_bf16 v[238:241], v[118:121], v[54:57], v[238:241]\n\t" \
  "v_mfma_f32_16x16x32_bf16 v[234:237], v[106:109], v[58:61], v[234:237]\n\tv_cndmask_b32_e32 v250, v227, v247, vcc\n\tv_cndmask_b32_e32 v251, v231, v227, vcc\n\t" \
  "v_mfma_f32_16x16x32_bf16 v[238:241], v[110:113], v[58:61], v[238:241]\n\ts_waitcnt lgkmcnt(15)\n\tv_mfma_f32_16x16x32_bf16 v[234:237], v[154:157], v[34:37], v[234:237]\n\t" \
  "v_add_f32_dpp v226, v250, v226 wave_ror:1 row_mask:0xf bank_mask:0xf bound_ctrl:1\n\tv_add_f32_dpp v230, v251, v230 wave_ror:1 row_mask:0xf bank_mask:0xf bound_ctrl:1\n\tv_mfma_f32_16x16x32_bf16 v[238:241], v[158:161], v[34:37], v[238:241]\n\t" \
  "v_mfma_f32_16x16x32_bf16 v[234:237], v[98:101], v[62:65], v[234:237]\n\tv_cvt_pk_bf16_f32 v254, v226, v230\n\tv_mfma_f32_16x16x32_bf16 v[238:241], v[102:105], v[62:65], v[238:241]\n\t" \
  "v_mfma_f32_16x16x32_bf16 v[242:245], v[154:157], v[38:41], 0\n\tds_read_b64_tr_b16 v[78:79], %8 offset:2816\n\tds_read_b64_tr_b16 v[80:81], %8 offset:2848\n\t" \
  "v_mfma_f32_16x16x32_bf16 v[246:249], v[158:161], v[38:41], 0\n\tds_read_b64_tr_b16 v[186:187], %4 offset:11264\n\tds_read_b64_tr_b16 v[188:189], %5 offset:11264\n\t" \
  "v_mfma_f32_16x16x32_bf16 v[242:245], v[146:149], v[42:45], v[242:245]\n\tds_read_b64_tr_b16 v[190:191], %6 offset:11264\n\tds_read_b64_tr_b16 v[192:193], %7 offset:11264\n\t" \
  "v_mfma_f32_16x16x32_bf16 v[246:249], v[150:153], v[42:45], v[246:249]\n\tv_cndmask_b32_e32 v250, v237, v233, vcc\n\tv_cndmask_b32_e32 v251, v241, v237, vcc\n\t" \
  "v_mfma_f32_16x16x32_bf16 v[242:245], v[138:141], v[46:49], v[242:245]\n\tv_add_f32_dpp v236, v250, v236 wave_ror:1 row_mask:0xf bank_mask:0xf bound_ctrl:1\n\tv_add_f32_dpp v240, v251, v240 wave_ror:1 row_mask:0xf bank_mask:0xf bound_ctrl:1\n\t" \
  "v_mfma_f32_16x16x32_bf16 v[246:249], v[142:145], v[46:49], v[246:249]\n\tv_cndmask_b32_e32 v250, v236, v232, vcc\n\tv_cndmask_b32_e32 v251, v240, v236, vcc\n\t" \
  "v_mfma_f32_16x16x32_bf16 v[242:245], v[130:133], v[50:53], v[242:245]\n\tv_mfma_f32_16x16x32_bf16 v[246:249], v[134:137], v[50:53], v[246:249]\n\tv_add_f32_dpp v235, v250, v235 wave_ror:1 row_mask:0xf bank_mask:0xf bound_ctrl:1\n\t" \
  "v_add_f32_dpp v239, v251, v239 wave_ror:1 row_mask:0xf bank_mask:0xf bound_ctrl:1\n\tv_mfma_f32_16x16x32_bf16 v[242:245], v[122:125], v[54:57], v[242:245]\n\tv_cndmask_b32_e32 v250, v235, v231, vcc\n\t" \
  "v_cndmask_b32_e32 v251, v239, v235, vcc\n\tv_mfma_f32_16x16x32_bf16 v[246:249], v[126:129], v[54:57], v[246:249]\n\tv_add_f32_dpp v234, v250, v234 wave_ror:1 row_mask:0xf bank_mask:0xf bound_ctrl:1\n\t" \
  "v_add_f32_dpp v238, v251, v238 wave_ror:1 row_mask:0xf bank_mask:0xf bound_ctrl:1\n\tv_mfma_f32_16x16x32_bf16 v[242:245], v[114:117], v[58:61], v[242:245]\n\tv_mfma_f32_16x16x32_bf16 v[246:249], v[118:121], v[58:61], v[246:249]\n\t" \
  "v_cvt_pk_bf16_f32 v255, v234, v238\n\tbuffer_store_dwordx4 v[252:255], %9, %13, 0 offen offset:1024 sc1\n\tv_mfma_f32_16x16x32_bf16 v[242:245], v[106:109], v[62:65], v[242:245]\n\t" \
  "s_mov_b64 s[40:41], %16\n\tglobal_load_dwordx4 %0, %11, s[40:41]\n\tv_mfma_f32_16x16x32_bf16 v[246:249], v[110:113], v[62:65], v[246:249]\n\t" \
  "s_add_u32 s40, s40, 0x20000\n\ts_addc_u32 s41, s41, 0\n\tglobal_load_dwordx4 %1, %11, s[40:41]\n\t" \
  "s_waitcnt lgkmcnt(15)\n\tv_mfma_f32_16x16x32_bf16 v[242:245], v[162:165], v[34:37], v[242:245]\n\tv_mfma_f32_16x16x32_bf16 v[246:249], v[166:169], v[34:37], v[246:249]\n\t" \
  "s_add_u32 s40, s40, 0x20000\n\ts_addc_u32 s41, s41, 0\n\tglobal_load_dwordx4 %2, %11, s[40:41]\n\t" \
  "v_mfma_f32_16x16x32_bf16 v[242:245], v[98:101], v[66:69], v[242:245]\n\ts_add_u32 s40, s40, 0x20000\n\ts_addc_u32 s41, s41, 0\n\t" \
  "global_load_dwordx4 %3, %11, s[40:41]\n\tv_mfma_f32_16x16x32_bf16 v[246:249], v[102:105], v[66:69], v[246:249]\n\tv_mfma_f32_16x16x32_bf16 v[226:229], v[162:165], v[38:41], 0\n\t" \
  "ds_read_b64_tr_b16 v[82:83], %8 offset:3072\n\tds_read_b64_tr_b16 v[84:85], %8 offset:3104\n\tv_mfma_f32_16x16x32_bf16 v[230:233], v[166:169], v[38:41], 0\n\t" \
  "ds_read_b64_tr_b16 v[194:195], %4 offset:12288\n\tds_read_b64_tr_b16 v[196:197], %5 offset:12288\n\tv_mfma_f32_16x16x32_bf16 v[226:229], v[154:157], v[42:45], v[226:229]\n\t" \
  "ds_read_b64_tr_b16 v[198:199], %6 offset:12288\n\tds_read_b64_tr_b16 v[200:201], %7 offset:12288\n\tv_mfma_f32_16x16x32_bf16 v[230:233], v[158:161], v[42:45], v[230:233]\n\t" \
  "v_cndmask_b32_e32 v250, v245, v241, vcc\n\tv_cndmask_b32_e32 v251, v249, v245, vcc\n\tv_mfma_f32_16x16x32_bf16 v[226:229], v[146:149], v[46:49], v[226:229]\n\t" \
  "v_mfma_f32_16x16x32_bf16 v[230:233], v[150:153], v[46:49], v[230:233]\n\tv_add_f32_dpp v244, v250, v244 wave_ror:1 row_mask:0xf bank_mask:0xf bound_ctrl:1\n\tv_add_f32_dpp v248, v251, v248 wave_ror:1 row_mask:0xf bank_mask:0xf bound_ctrl:1\n\t" \
  "v_mfma_f32_16x16x32_bf16 v[226:229], v[138:141], v[50:53], v[226:229]\n\tv_mfma_f32_16x16x32_bf16 v[230:233], v[142:145], v[50:53], v[230:233]\n\tv_cndmask_b32_e32 v250, v244, v240, vcc\n\t" \
  "v_cndmask_b32_e32 v251, v248, v244, vcc\n\tv_mfma_f32_16x16x32_bf16 v[226:229], v[130:133], v[54:57], v[226:229]\n\tv_mfma_f32_16x16x32_bf16 v[230:233], v[134:137], v[54:57], v[230:233]\n\t" \
  "v_mfma_f32_16x16x32_bf16 v[226:229], v[122:125], v[58:61], v[226:229]\n\tv_add_f32_dpp v243, v250, v243 wave_ror:1 row_mask:0xf bank_mask:0xf bound_ctrl:1\n\tv_add_f32_dpp v247, v251, v247 wave_ror:1 row_mask:0xf bank_mask:0xf bound_ctrl:1\n\t" \
  "v_mfma_f32_16x16x32_bf16 v[230:233], v[126:129], v[58:61], v[230:233]\n\tv_mfma_f32_16x16x32_bf16 v[226:229], v[114:117], v[62:65], v[226:229]\n\tv_cndmask_b32_e32 v250, v243, v239, vcc\n\t" \
  "v_cndmask_b32_e32 v251, v247, v243, vcc\n\tv_mfma_f32_16x16x32_bf16 v[230:233], v[118:121], v[62:65], v[230:233]\n\tv_mfma_f32_16x16x32_bf16 v[226:229], v[106:109], v[66:69], v[226:229]\n\t" \
  "v_mfma_f32_16x16x32_bf16 v[230:233], v[110:113], v[66:69], v[230:233]\n\tv_add_f32_dpp v242, v250, v242 wave_ror:1 row_mask:0xf bank_mask:0xf bound_ctrl:1\n\tv_add_f32_dpp v246, v251, v246 wave_ror:1 row_mask:0xf bank_mask:0xf bound_ctrl:1\n\t" \
  "s_waitcnt lgkmcnt(15)\n\tv_mfma_f32_16x16x32_bf16 v[226:229], v[170:173], v[34:37], v[226:229]\n\tv_mfma_f32_16x16x32_bf16 v[230:233], v[174:177], v[34:37], v[230:233]\n\t" \
  "v_cvt_pk_bf16_f32 v252, v242, v246\n\tv_mfma_f32_16x16x32_bf16 v[226:229], v[98:101], v[70:73], v[226:229]\n\tv_mfma_f32_16x16x32_bf16 v[230:233], v[102:105], v[70:73], v[230:233]\n\t" \
  "v_mfma_f32_16x16x32_bf16 v[234:237], v[170:173], v[38:41], 0\n\tds_read_b64_tr_b16 v[86:87], %8 offset:3328\n\tds_read_b64_tr_b16 v[88:89], %8 offset:3360\n\t" \
  "v_mfma_f32_16x16x32_bf16 v[238:241], v[174:177], v[38:41], 0\n\tds_read_b64_tr_b16 v[202:203], %4 offset:13312\n\tds_read_b64_tr_b16 v[204:205], %5 offset:13312\n\t" \
  "v_mfma_f32_16x16x32_bf16 v[234:237], v[162:165], v[42:45], v[234:237]\n\tds_read_b64_tr_b16 v[206:207], %6 offset:13312\n\tds_read_b64_tr_b16 v[208:209], %7 offset:13312\n\t" \
  "v_mfma_f32_16x16x32_bf16 v[238:241], v[166:169], v[42:45], v[238:241]\n\tv_cndmask_b32_e32 v250, v229, v249, vcc\n\tv_cndmask_b32_e32 v251, v233, v229, vcc\n\t" \
  "v_mfma_f32_16x16x32_bf16 v[234:237], v[154:157], v[46:49], v[234:237]\n\tv_mfma_f32_16x16x32_bf16 v[238:241], v[158:161], v[46:49], v[238:241]\n\tv_add_f32_dpp v228, v250, v228 wave_ror:1 row_mask:0xf bank_mask:0xf bound_ctrl:1\n\t" \
  "v_add_f32_dpp v232, v251, v232 wave_ror:1 row_mask:0xf bank_mask:0xf bound_ctrl:1\n\tv_mfma_f32_16x16x32_bf16 v[234:237], v[146:149], v[50:53], v[234:237]\n\tv_mfma_f32_16x16x32_bf16 v[238:241], v[150:153], v[50:53], v[238:241]\n\t" \
  "v_mfma_f32_16x16x32_bf16 v[234:237], v[138:141], v[54:57], v[234:237]\n\tv_cndmask_b32_e32 v250, v228, v248, vcc\n\tv_cndmask_b32_e32 v251, v232, v228, vcc\n\t" \
  "v_mfma_f32_16x16x32_bf16 v[238:241], v[142:145], v[54:57], v[238:241]\n\tv_mfma_f32_16x16x32_bf16 v[234:237], v[130:133], v[58:61], v[234:237]\n\tv_mfma_f32_16x16x32_bf16 v[238:241], v[134:137], v[58:61], v[238:241]\n\t" \
  "v_add_f32_dpp v227, v250, v227 wave_ror:1 row_mask:0xf bank_mask:0xf bound_ctrl:1\n\tv_add_f32_dpp v231, v251, v231 wave_ror:1 row_mask:0xf bank_mask:0xf bound_ctrl:1\n\tv_mfma_f32_16x16x32_bf16 v[234:237], v[122:125], v[62:65], v[234:237]\n\t" \
  "v_mfma_f32_16x16x32_bf16 v[238:241], v[126:129], v[62:65], v[238:241]\n\tv_cndmask_b32_e32 v250, v227, v247, vcc\n\tv_cndmask_b32_e32 v251, v231, v227, vcc\n\t" \
  "v_mfma_f32_16x16x32_bf16 v[234:237], v[114:117], v[66:69], v[234:237]\n\tv_mfma_f32_16x16x32_bf16 v[238:241], v[118:121], v[66:69], v[238:241]\n\tv_mfma_f32_16x16x32_bf16 v[234:237], v[106:109], v[70:73], v[234:237]\n\t" \
  "v_add_f32_dpp v226, v250, v226 wave_ror:1 row_mask:0xf bank_mask:0xf bound_ctrl:1\n\tv_add_f32_dpp v230, v251, v230 wave_ror:1 row_mask:0xf bank_mask:0xf bound_ctrl:1\n\tv_mfma_f32_16x16x32_bf16 v[238:241], v[110:113], v[70:73], v[238:241]\n\t" \
  "s_waitcnt lgkmcnt(15)\n\tv_mfma_f32_16x16x32_bf16 v[234:237], v[178:181], v[34:37], v[234:237]\n\tv_mfma_f32_16x16x32_bf16 v[238:241], v[182:185], v[34:37], v[238:241]\n\t" \
  "v_cvt_pk_bf16_f32 v253, v226, v230\n\tv_mfma_f32_16x16x32_bf16 v[234:237], v[98:101], v[74:77], v[234:237]\n\tv_mfma_f32_16x16x32_bf16 v[238:241], v[102:105], v[74:77], v[238:241]\n\t" \
  "v_mfma_f32_16x16x32_bf16 v[242:245], v[178:181], v[38:41], 0\n\tds_read_b64_tr_b16 v[90:91], %8 offset:3584\n\tds_read_b64_tr_b16 v[92:93], %8 offset:3616\n\t" \
  "v_mfma_f32_16x16x32_bf16 v[246:249], v[182:185], v[38:41], 0\n\tds_read_b64_tr_b16 v[210:211], %4 offset:14336\n\tds_read_b64_tr_b16 v[212:213], %5 offset:14336\n\t" \
  "v_mfma_f32_16x16x32_bf16 v[242:245], v[170:173], v[42:45], v[242:245]\n\tds_read_b64_tr_b16 v[214:215], %6 offset:14336\n\tds_read_b64_tr_b16 v[216:217], %7 offset:14336\n\t" \
  "v_mfma_f32_16x16x32_bf16 v[246:249], v[174:177], v[42:45], v[246:249]\n\tv_cndmask_b32_e32 v250, v237, v233, vcc\n\tv_cndmask_b32_e32 v251, v241, v237, vcc\n\t" \
  "v_mfma_f32_16x16x32_bf16 v[242:245], v[162:165], v[46:49], v[242:245]\n\tv_mfma_f32_16x16x32_bf16 v[246:249], v[166:169], v[46:49], v[246:249]\n\tv_mfma_f32_16x16x32_bf16 v[242:245], v[154:157], v[50:53], v[242:245]\n\t" \
  "v_add_f32_dpp v236, v250, v236 wave_ror:1 row_mask:0xf bank_mask:0xf bound_ctrl:1\n\tv_add_f32_dpp v240, v251, v240 wave_ror:1 row_mask:0xf bank_mask:0xf bound_ctrl:1\n\tv_mfma_f32_16x16x32_bf16 v[246:249], v[158:161], v[50:53], v[246:249]\n\t" \
  "v_mfma_f32_16x16x32_bf16 v[242:245], v[146:149], v[54:57], v[242:245]\n\tv_mfma_f32_16x16x32_bf16 v[246:249], v[150:153], v[54:57], v[246:249]\n\tv_cndmask_b32_e32 v250, v236, v232, vcc\n\t" \
  "v_cndmask_b32_e32 v251, v240, v236, vcc\n\tv_mfma_f32_16x16x32_bf16 v[242:245], v[138:141], v[58:61], v[242:245]\n\tv_mfma_f32_16x16x32_bf16 v[246:249], v[142:145], v[58:61], v[246:249]\n\t" \
  "v_mfma_f32_16x16x32_bf16 v[242:245], v[130:133], v[62:65], v[242:245]\n\tv_add_f32_dpp v235, v250, v235 wave_ror:1 row_mask:0xf bank_mask:0xf bound_ctrl:1\n\tv_add_f32_dpp v239, v251, v239 wave_ror:1 row_mask:0xf bank_mask:0xf bound_ctrl:1\n\t" \
  "v_mfma_f32_16x16x32_bf16 v[246:249], v[134:137], v[62:65], v[246:249]\n\tv_mfma_f32_16x16x32_bf16 v[242:245], v[122:125], v[66:69], v[242:245]\n\tv_mfma_f32_16x16x32_bf16 v[246:249], v[126:129], v[66:69], v[246:249]\n\t" \
  "v_cndmask_b32_e32 v250, v235, v231, vcc\n\tv_cndmask_b32_e32 v251, v239, v235, vcc\n\tv_mfma_f32_16x16x32_bf16 v[242:245], v[114:117], v[70:73], v[242:245]\n\t" \
  "v_mfma_f32_16x16x32_bf16 v[246:249], v[118:121], v[70:73], v[246:249]\n\tv_mfma_f32_16x16x32_bf16 v[242:245], v[106:109], v[74:77], v[242:245]\n\tv_add_f32_dpp v234, v250, v234 wave_ror:1 row_mask:0xf bank_mask:0xf bound_ctrl:1\n\t" \
  "v_add_f32_dpp v238, v251, v238 wave_ror:1 row_mask:0xf bank_mask:0xf bound_ctrl:1\n\tv_mfma_f32_16x16x32_bf16 v[246:249], v[110:113], v[74:77], v[246:249]\n\ts_waitcnt lgkmcnt(15)\n\t" \
  "v_mfma_f32_16x16x32_bf16 v[242:245], v[186:189], v[34:37], v[242:245]\n\tv_mfma_f32_16x16x32_bf16 v[246:249], v[190:193], v[34:37], v[246:249]\n\tv_cvt_pk_bf16_f32 v254, v234, v238\n\t" \
  "v_mfma_f32_16x16x32_bf16 v[242:245], v[98:101], v[78:81], v[242:245]\n\tv_mfma_f32_16x16x32_bf16 v[246:249], v[102:105], v[78:81], v[246:249]\n\tv_mfma_f32_16x16x32_bf16 v[226:229], v[186:189], v[38:41], 0\n\t" \
  "ds_read_b64_tr_b16 v[94:95], %8 offset:3840\n\tds_read_b64_tr_b16 v[96:97], %8 offset:3872\n\tv_mfma_f32_16x16x32_bf16 v[230:233], v[190:193], v[38:41], 0\n\t" \
  "ds_read_b64_tr_b16 v[218:219], %4 offset:15360\n\tds_read_b64_tr_b16 v[220:221], %5 offset:15360\n\tv_mfma_f32_16x16x32_bf16 v[226:229], v[178:181], v[42:45], v[226:229]\n\t" \
  "ds_read_b64_tr_b16 v[222:223], %6 offset:15360\n\tds_read_b64_tr_b16 v[224:225], %7 offset:15360\n\tv_mfma_f32_16x16x32_bf16 v[230:233], v[182:185], v[42:45], v[230:233]\n\t" \
  "v_mov_b32 v250, 1\n\ts_mov_b64 exec, 1\n\tds_write_b32 %12, v250 offset:48\n\t" \
  "s_mov_b64 exec, -1\n\tv_mfma_f32_16x16x32_bf16 v[226:229], v[170:173], v[46:49], v[226:229]\n\tv_cndmask_b32_e32 v250, v245, v241, vcc\n\t" \
  "v_cndmask_b32_e32 v251, v249, v245, vcc\n\tv_mfma_f32_16x16x32_bf16 v[230:233], v[174:177], v[46:49], v[230:233]\n\tv_mfma_f32_16x16x32_bf16 v[226:229], v[162:165], v[50:53], v[226:229]\n\t" \
  "v_mfma_f32_16x16x32_bf16 v[230:233], v[166:169], v[50:53], v[230:233]\n\tv_add_f32_dpp v244, v250, v244 wave_ror:1 row_mask:0xf bank_mask:0xf bound_ctrl:1\n\tv_add_f32_dpp v248, v251, v248 wave_ror:1 row_mask:0xf bank_mask:0xf bound_ctrl:1\n\t" \
  "v_mfma_f32_16x16x32_bf16 v[226:229], v[154:157], v[54:57], v[226:229]\n\tv_mfma_f32_16x16x32_bf16 v[230:233], v[158:161], v[54:57], v[230:233]\n\tv_mfma_f32_16x16x32_bf16 v[226:229], v[146:149], v[58:61], v[226:229]\n\t" \
  "v_cndmask_b32_e32 v250, v244, v240, vcc\n\tv_cndmask_b32_e32 v251, v248, v244, vcc\n\tv_mfma_f32_16x16x32_bf16 v[230:233], v[150:153], v[58:61], v[230:233]\n\t" \
  "v_mfma_f32_16x16x32_bf16 v[226:229], v[138:141], v[62:65], v[226:229]\n\tv_mfma_f32_16x16x32_bf16 v[230:233], v[142:145], v[62:65], v[230:233]\n\tv_add_f32_dpp v243, v250, v243 wave_ror:1 row_mask:0xf bank_mask:0xf bound_ctrl:1\n\t" \
  "v_add_f32_dpp v247, v251, v247 wave_ror:1 row_mask:0xf bank_mask:0xf bound_ctrl:1\n\tv_mfma_f32_16x16x32_bf16 v[226:229], v[130:133], v[66:69], v[226:229]\n\tv_mfma_f32_16x16x32_bf16 v[230:233], v[134:137], v[66:69], v[230:233]\n\t" \
  "v_mfma_f32_16x16x32_bf16 v[226:229], v[122:125], v[70:73], v[226:229]\n\tv_cndmask_b32_e32 v250, v243, v239, vcc\n\tv_cndmask_b32_e32 v251, v247, v243, vcc\n\t" \
  "v_mfma_f32_16x16x32_bf16 v[230:233], v[126:129], v[70:73], v[230:233]\n\tv_mfma_f32_16x16x32_bf16 v[226:229], v[114:117], v[74:77], v[226:229]\n\tv_mfma_f32_16x16x32_bf16 v[230:233], v[118:121], v[74:77], v[230:233]\n\t" \
  "v_add_f32_dpp v242, v250, v242 wave_ror:1 row_mask:0xf bank_mask:0xf bound_ctrl:1\n\tv_add_f32_dpp v246, v251, v246 wave_ror:1 row_mask:0xf bank_mask:0xf bound_ctrl:1\n\tv_mfma_f32_16x16x32_bf16 v[226:229], v[106:109], v[78:81], v[226:229]\n\t" \
  "v_mfma_f32_16x16x32_bf16 v[230:233], v[110:113], v[78:81], v[230:233]\n\ts_waitcnt lgkmcnt(15)\n\tv_mfma_f32_16x16x32_bf16 v[226:229], v[194:197], v[34:37], v[226:229]\n\t" \
  "v_cvt_pk_bf16_f32 v255, v242, v246\n\tbuffer_store_dwordx4 v[252:255], %9, %13, 0 offen offset:2048 sc1\n\tv_mfma_f32_16x16x32_bf16 v[230:233], v[198:201], v[34:37], v[230:233]\n\t" \
  "v_mfma_f32_16x16x32_bf16 v[226:229], v[98:101], v[82:85], v[226:229]\n\tv_mfma_f32_16x16x32_bf16 v[230:233], v[102:105], v[82:85], v[230:233]\n\tv_mfma_f32_16x16x32_bf16 v[234:237], v[194:197], v[38:41], 0\n\t" \
  "v_mfma_f32_16x16x32_bf16 v[238:241], v[198:201], v[38:41], 0\n\tv_mfma_f32_16x16x32_bf16 v[234:237], v[186:189], v[42:45], v[234:237]\n\tv_mfma_f32_16x16x32_bf16 v[238:241], v[190:193], v[42:45], v[238:241]\n\t" \
  "s_nop 2\n\tv_cndmask_b32_e32 v250, v229, v249, vcc\n\tv_cndmask_b32_e32 v251, v233, v229, vcc\n\t" \
  "v_mfma_f32_16x16x32_bf16 v[234:237], v[178:181], v[46:49], v[234:237]\n\tv_mfma_f32_16x16x32_bf16 v[238:241], v[182:185], v[46:49], v[238:241]\n\tv_mfma_f32_16x16x32_bf16 v[234:237], v[170:173], v[50:53], v[234:237]\n\t" \
  "v_add_f32_dpp v228, v250, v228 wave_ror:1 row_mask:0xf bank_mask:0xf bound_ctrl:1\n\tv_add_f32_dpp v232, v251, v232 wave_ror:1 row_mask:0xf bank_mask:0xf bound_ctrl:1\n\tv_mfma_f32_16x16x32_bf16 v[238:241], v[174:177], v[50:53], v[238:241]\n\t" \
  "v_mfma_f32_16x16x32_bf16 v[234:237], v[162:165], v[54:57], v[234:237]\n\tv_mfma_f32_16x16x32_bf16 v[238:241], v[166:169], v[54:57], v[238:241]\n\tv_mfma_f32_16x16x32_bf16 v[234:237], v[154:157], v[58:61], v[234:237]\n\t" \
  "v_cndmask_b32_e32 v250, v228, v248, vcc\n\tv_cndmask_b32_e32 v251, v232, v228, vcc\n\tv_mfma_f32_16x16x32_bf16 v[238:241], v[158:161], v[58:61], v[238:241]\n\t" \
  "v_mfma_f32_16x16x32_bf16 v[234:237], v[146:149], v[62:65], v[234:237]\n\tv_mfma_f32_16x16x32_bf16 v[238:241], v[150:153], v[62:65], v[238:241]\n\tv_add_f32_dpp v227, v250, v227 wave_ror:1 row_mask:0xf bank_mask:0xf bound_ctrl:1\n\t" \
  "v_add_f32_dpp v231, v251, v231 wave_ror:1 row_mask:0xf bank_mask:0xf bound_ctrl:1\n\tv_mfma_f32_16x16x32_bf16 v[234:237], v[138:141], v[66:69], v[234:237]\n\tv_mfma_f32_16x16x32_bf16 v[238:241], v[142:145], v[66:69], v[238:241]\n\t" \
  "v_mfma_f32_16x16x32_bf16 v[234:237], v[130:133], v[70:73], v[234:237]\n\tv_mfma_f32_16x16x32_bf16 v[238:241], v[134:137], v[70:73], v[238:241]\n\tv_cndmask_b32_e32 v250, v227, v247, vcc\n\t" \
  "v_cndmask_b32_e32 v251, v231, v227, vcc\n\tv_mfma_f32_16x16x32_bf16 v[234:237], v[122:125], v[74:77], v[234:237]\n\tv_mfma_f32_16x16x32_bf16 v[238:241], v[126:129], v[74:77], v[238:241]\n\t" \
  "v_mfma_f32_16x16x32_bf16 v[234:237], v[114:117], v[78:81], v[234:237]\n\tv_add_f32_dpp v226, v250, v226 wave_ror:1 row_mask:0xf bank_mask:0xf bound_ctrl:1\n\tv_add_f32_dpp v230, v251, v230 wave_ror:1 row_mask:0xf bank_mask:0xf bound_ctrl:1\n\t" \
  "v_mfma_f32_16x16x32_bf16 v[238:241], v[118:121], v[78:81], v[238:241]\n\tv_mfma_f32_16x16x32_bf16 v[234:237], v[106:109], v[82:85], v[234:237]\n\tv_mfma_f32_16x16x32_bf16 v[238:241], v[110:113], v[82:85], v[238:241]\n\t" \
  "v_mfma_f32_16x16x32_bf16 v[234:237], v[202:205], v[34:37], v[234:237]\n\tv_cvt_pk_bf16_f32 v252, v226, v230\n\ts_waitcnt lgkmcnt(13)\n\t" \
  "v_mfma_f32_16x16x32_bf16 v[238:241], v[206:209], v[34:37], v[238:241]\n\tv_mfma_f32_16x16x32_bf16 v[234:237], v[98:101], v[86:89], v[234:237]\n\tv_mfma_f32_16x16x32_bf16 v[238:241], v[102:105], v[86:89], v[238:241]\n\t" \
  "v_mfma_f32_16x16x32_bf16 v[242:245], v[202:205], v[38:41], 0\n\tv_mfma_f32_16x16x32_bf16 v[246:249], v[206:209], v[38:41], 0\n\tv_mfma_f32_16x16x32_bf16 v[242:245], v[194:197], v[42:45], v[242:245]\n\t" \
  "v_mfma_f32_16x16x32_bf16 v[246:249], v[198:201], v[42:45], v[246:249]\n\ts_nop 2\n\tv_cndmask_b32_e32 v250, v237, v233, vcc\n\t" \
  "v_cndmask_b32_e32 v251, v241, v237, vcc\n\tv_mfma_f32_16x16x32_bf16 v[242:245], v[186:189], v[46:49], v[242:245]\n\tv_mfma_f32_16x16x32_bf16 v[246:249], v[190:193], v[46:49], v[246:249]\n\t" \
  "v_mfma_f32_16x16x32_bf16 v[242:245], v[178:181], v[50:53], v[242:245]\n\tv_add_f32_dpp v236, v250, v236 wave_ror:1 row_mask:0xf bank_mask:0xf bound_ctrl:1\n\tv_add_f32_dpp v240, v251, v240 wave_ror:1 row_mask:0xf bank_mask:0xf bound_ctrl:1\n\t" \
  "v_mfma_f32_16x16x32_bf16 v[246:249], v[182:185], v[50:53], v[246:249]\n\tv_mfma_f32_16x16x32_bf16 v[242:245], v[170:173], v[54:57], v[242:245]\n\tv_mfma_f32_16x16x32_bf16 v[246:249], v[174:177], v[54:57], v[246:249]\n\t" \
  "v_mfma_f32_16x16x32_bf16 v[242:245], v[162:165], v[58:61], v[242:245]\n\tv_cndmask_b32_e32 v250, v236, v232, vcc\n\tv_cndmask_b32_e32 v251, v240, v236, vcc\n\t" \
  "v_mfma_f32_16x16x32_bf16 v[246:249], v[166:169], v[58:61], v[246:249]\n\tv_mfma_f32_16x16x32_bf16 v[242:245], v[154:157], v[62:65], v[242:245]\n\tv_mfma_f32_16x16x32_bf16 v[246:249], v[158:161], v[62:65], v[246:249]\n\t" \
  "v_mfma_f32_16x16x32_bf16 v[242:245], v[146:149], v[66:69], v[242:245]\n\tv_add_f32_dpp v235, v250, v235 wave_ror:1 row_mask:0xf bank_mask:0xf bound_ctrl:1\n\tv_add_f32_dpp v239, v251, v239 wave_ror:1 row_mask:0xf bank_mask:0xf bound_ctrl:1\n\t" \
  "v_mfma_f32_16x16x32_bf16 v[246:249], v[150:153], v[66:69], v[246:249]\n\tv_mfma_f32_16x16x32_bf16 v[242:245], v[138:141], v[70:73], v[242:245]\n\tv_mfma_f32_16x16x32_bf16 v[246:249], v[142:145], v[70:73], v[246:249]\n\t" \
  "v_mfma_f32_16x16x32_bf16 v[242:245], v[130:133], v[74:77], v[242:245]\n\tv_cndmask_b32_e32 v250, v235, v231, vcc\n\tv_cndmask_b32_e32 v251, v239, v235, vcc\n\t" \
  "v_mfma_f32_16x16x32_bf16 v[246:249], v[134:137], v[74:77], v[246:249]\n\tv_mfma_f32_16x16x32_bf16 v[242:245], v[122:125], v[78:81], v[242:245]\n\tv_mfma_f32_16x16x32_bf16 v[246:249], v[126:129], v[78:81], v[246:249]\n\t" \
  "v_mfma_f32_16x16x32_bf16 v[242:245], v[114:117], v[82:85], v[242:245]\n\tv_add_f32_dpp v234, v250, v234 wave_ror:1 row_mask:0xf bank_mask:0xf bound_ctrl:1\n\tv_add_f32_dpp v238, v251, v238 wave_ror:1 row_mask:0xf bank_mask:0xf bound_ctrl:1\n\t" \
  "v_mfma_f32_16x16x32_bf16 v[246:249], v[118:121], v[82:85], v[246:249]\n\tv_mfma_f32_16x16x32_bf16 v[242:245], v[106:109], v[86:89], v[242:245]\n\tv_mfma_f32_16x16x32_bf16 v[246:249], v[110:113], v[86:89], v[246:249]\n\t" \
  "s_waitcnt lgkmcnt(9)\n\tv_mfma_f32_16x16x32_bf16 v[242:245], v[210:213], v[34:37], v[242:245]\n\tv_cvt_pk_bf16_f32 v253, v234, v238\n\t" \
  "s_waitcnt lgkmcnt(7)\n\tv_mfma_f32_16x16x32_bf16 v[246:249], v[214:217], v[34:37], v[246:249]\n\tv_mfma_f32_16x16x32_bf16 v[242:245], v[98:101], v[90:93], v[242:245]\n\t" \
  "v_mfma_f32_16x16x32_bf16 v[246:249], v[102:105], v[90:93], v[246:249]\n\tv_mfma_f32_16x16x32_bf16 v[226:229], v[210:213], v[38:41], 0\n\tv_mfma_f32_16x16x32_bf16 v[230:233], v[214:217], v[38:41], 0\n\t" \
  "v_mfma_f32_16x16x32_bf16 v[226:229], v[202:205], v[42:45], v[226:229]\n\tv_mfma_f32_16x16x32_bf16 v[230:233], v[206:209], v[42:45], v[230:233]\n\ts_nop 2\n\t" \
  "v_cndmask_b32_e32 v250, v245, v241, vcc\n\tv_cndmask_b32_e32 v251, v249, v245, vcc\n\tv_mfma_f32_16x16x32_bf16 v[226:229], v[194:197], v[46:49], v[226:229]\n\t" \
  "v_mfma_f32_16x16x32_bf16 v[230:233], v[198:201], v[46:49], v[230:233]\n\tv_mfma_f32_16x16x32_bf16 v[226:229], v[186:189], v[50:53], v[226:229]\n\tv_mfma_f32_16x16x32_bf16 v[230:233], v[190:193], v[50:53], v[230:233]\n\t" \
  "v_add_f32_dpp v244, v250, v244 wave_ror:1 row_mask:0xf bank_mask:0xf bound_ctrl:1\n\tv_add_f32_dpp v248, v251, v248 wave_ror:1 row_mask:0xf bank_mask:0xf bound_ctrl:1\n\tv_mfma_f32_16x16x32_bf16 v[226:229], v[178:181], v[54:57], v[226:229]\n\t" \
  "v_mfma_f32_16x16x32_bf16 v[230:233], v[182:185], v[54:57], v[230:233]\n\tv_mfma_f32_16x16x32_bf16 v[226:229], v[170:173], v[58:61], v[226:229]\n\tv_mfma_f32_16x16x32_bf16 v[230:233], v[174:177], v[58:61], v[230:233]\n\t" \
  "v_cndmask_b32_e32 v250, v244, v240, vcc\n\tv_cndmask_b32_e32 v251, v248, v244, vcc\n\tv_mfma_f32_16x16x32_bf16 v[226:229], v[162:165], v[62:65], v[226:229]\n\t" \
  "v_mfma_f32_16x16x32_bf16 v[230:233], v[166:169], v[62:65], v[230:233]\n\tv_mfma_f32_16x16x32_bf16 v[226:229], v[154:157], v[66:69], v[226:229]\n\tv_mfma_f32_16x16x32_bf16 v[230:233], v[158:161], v[66:69], v[230:233]\n\t" \
  "v_add_f32_dpp v243, v250, v243 wave_ror:1 row_mask:0xf bank_mask:0xf bound_ctrl:1\n\tv_add_f32_dpp v247, v251, v247 wave_ror:1 row_mask:0xf bank_mask:0xf bound_ctrl:1\n\tv_mfma_f32_16x16x32_bf16 v[226:229], v[146:149], v[70:73], v[226:229]\n\t" \
  "v_mfma_f32_16x16x32_bf16 v[230:233], v[150:153], v[70:73], v[230:233]\n\tv_mfma_f32_16x16x32_bf16 v[226:229], v[138:141], v[74:77], v[226:229]\n\tv_mfma_f32_16x16x32_bf16 v[230:233], v[142:145], v[74:77], v[230:233]\n\t" \
  "v_cndmask_b32_e32 v250, v243, v239, vcc\n\tv_cndmask_b32_e32 v251, v247, v243, vcc\n\tv_mfma_f32_16x16x32_bf16 v[226:229], v[130:133], v[78:81], v[226:229]\n\t" \
  "v_mfma_f32_16x16x32_bf16 v[230:233], v[134:137], v[78:81], v[230:233]\n\tv_mfma_f32_16x16x32_bf16 v[226:229], v[122:125], v[82:85], v[226:229]\n\tv_mfma_f32_16x16x32_bf16 v[230:233], v[126:129], v[82:85], v[230:233]\n\t" \
  "v_add_f32_dpp v242, v250, v242 wave_ror:1 row_mask:0xf bank_mask:0xf bound_ctrl:1\n\tv_add_f32_dpp v246, v251, v246 wave_ror:1 row_mask:0xf bank_mask:0xf bound_ctrl:1\n\tv_mfma_f32_16x16x32_bf16 v[226:229], v[114:117], v[86:89], v[226:229]\n\t" \
  "v_mfma_f32_16x16x32_bf16 v[230:233], v[118:121], v[86:89], v[230:233]\n\tv_mfma_f32_16x16x32_bf16 v[226:229], v[106:109], v[90:93], v[226:229]\n\tv_mfma_f32_16x16x32_bf16 v[230:233], v[110:113], v[90:93], v[230:233]\n\t" \
  "v_cvt_pk_bf16_f32 v254, v242, v246\n\ts_waitcnt lgkmcnt(3)\n\tv_mfma_f32_16x16x32_bf16 v[226:229], v[218:221], v[34:37], v[226:229]\n\t" \
  "s_waitcnt lgkmcnt(1)\n\tv_mfma_f32_16x16x32_bf16 v[230:233], v[222:225], v[34:37], v[230:233]\n\tv_mfma_f32_16x16x32_bf16 v[226:229], v[98:101], v[94:97], v[226:229]\n\t" \
  "v_mfma_f32_16x16x32_bf16 v[230:233], v[102:105], v[94:97], v[230:233]\n\ts_nop 7\n\tv_cndmask_b32_e32 v250, v229, v249, vcc\n\t" \
  "v_cndmask_b32_e32 v251, v233, v229, vcc\n\ts_nop 0\n\tv_add_f32_dpp v228, v250, v228 wave_ror:1 row_mask:0xf bank_mask:0xf bound_ctrl:1\n\t" \
  "v_add_f32_dpp v232, v251, v232 wave_ror:1 row_mask:0xf bank_mask:0xf bound_ctrl:1\n\tv_cndmask_b32_e32 v250, v228, v248, vcc\n\tv_cndmask_b32_e32 v251, v232, v228, vcc\n\t" \
  "s_nop 0\n\tv_add_f32_dpp v227, v250, v227 wave_ror:1 row_mask:0xf bank_mask:0xf bound_ctrl:1\n\tv_add_f32_dpp v231, v251, v231 wave_ror:1 row_mask:0xf bank_mask:0xf bound_ctrl:1\n\t" \
  "v_cndmask_b32_e32 v250, v227, v247, vcc\n\tv_cndmask_b32_e32 v251, v231, v227, vcc\n\ts_nop 0\n\t" \
  "v_add_f32_dpp v226, v250, v226 wave_ror:1 row_mask:0xf bank_mask:0xf bound_ctrl:1\n\tv_add_f32_dpp v230, v251, v230 wave_ror:1 row_mask:0xf bank_mask:0xf bound_ctrl:1\n\tv_cvt_pk_bf16_f32 v255, v226, v230\n\t" \
  "buffer_store_dwordx4 v[252:255], %9, %13, 0 offen offset:3072 sc1\n\ts_nop 1\n\t"
#define CONV_CLOBBERS "v34", "v35", "v36", "v37", "v38", "v39", "v40", "v41", "v42", "v43", "v44", "v45", "v46", "v47", "v48", "v49", "v50", "v51", "v52", "v53", "v54", "v55", "v56", "v57", "v58", "v59", "v60", "v61", "v62", "v63", "v64", "v65", "v66", "v67", "v68", "v69", "v70", "v71", "v72", "v73", "v74", "v75", "v76", "v77", "v78", "v79", "v80", "v81", "v82", "v83", "v84", "v85", "v86", "v87", "v88", "v89", "v90", "v91", "v92", "v93", "v94", "v95", "v96", "v97", "v98", "v99", "v100", "v101", "v102", "v103", "v104", "v105", "v106", "v107", "v108", "v109", "v110", "v111", "v112", "v113", "v114", "v115", "v116", "v117", "v118", "v119", "v120", "v121", "v122", "v123", "v124", "v125", "v126", "v127", "v128", "v129", "v130", "v131", "v132", "v133", "v134", "v135", "v136", "v137", "v138", "v139", "v140", "v141", "v142", "v143", "v144", "v145", "v146", "v147", "v148", "v149", "v150", "v151", "v152", "v153", "v154", "v155", "v156", "v157", "v158", "v159", "v160", "v161", "v162", "v163", "v164", "v165", "v166", "v167", "v168", "v169", "v170", "v171", "v172", "v173", "v174", "v175", "v176", "v177", "v178", "v179", "v180", "v181", "v182", "v183", "v184", "v185", "v186", "v187", "v188", "v189", "v190", "v191", "v192", "v193", "v194", "v195", "v196", "v197", "v198", "v199", "v200", "v201", "v202", "v203", "v204", "v205", "v206", "v207", "v208", "v209", "v210", "v211", "v212", "v213", "v214", "v215", "v216", "v217", "v218", "v219", "v220", "v221", "v222", "v223", "v224", "v225", "v226", "v227", "v228", "v229", "v230", "v231", "v232", "v233", "v234", "v235", "v236", "v237", "v238", "v239", "v240", "v241", "v242", "v243", "v244", "v245", "v246", "v247", "v248", "v249", "v250", "v251", "v252", "v253", "v254", "v255"
#define G1_K0 12
#define A1_START 3
#define A1_PER 23
#define A1_LAG 2
#define A1_STAG 3
#define A1_ROWS 11
#define A1_DLY 8
#define H_AT 10
#define Q_AT 11
#define T_AT 99
#define A_LAG 0
#define XQ_AT 16
#define XQ_LAG 1
#define MK_AT 16
#define X_LAG 1
#define NTOUCH 3
#define TOUCH_DLY 6
#define PF_ON 0
#define LORD 0
#define PFD 0
#define BAR16_AT -1
#define WH_AT 8
#define GATE F_X1
#define ROTM 0
#define YSLEEP 6
__device__ __forceinline__ void helper_wave(const Params& P, lptr smem, int hw, int lane, int xl, int idx) {
  PAD_DECL(1)
  constexpr int LAST_ = 9;
  ST_DECL
  STAMP(0);
  const int hp = (xl & 3) * 4 + (idx & 3), rb = (xl >> 2) * 8 + (idx >> 2);
  const int hq = hp ^ QSWAP;
  const int nt = G2MAP ? ((idx & 15) ^ NTX) : 8 * (xl & 1) + ((idx & 7) ^ NTX);
  const unsigned voW = wo_voff(lane);
  const int ch = 128 * xl + 64 * (idx >> 4) + 8 * (4 * (idx & 1) + hw) + ((idx >> 1) & 7);
  unsigned dw;
  u32x4 qv[4];
  auto hburst = [&]() {
    const int i2 = 2 * (hw & 1);
    const char* hsrc = (const char*)P.ssm_k + (size_t)(128 * xl + 64 * (idx >> 4) + 8 * (4 * (idx & 1) + i2) + ((idx >> 1) & 7)) * 4096;
    const unsigned vl = lane * 16;
#pragma unroll
    for (int i = 0; i < 2; ++i) dma4(hsrc + i * 8 * 4096, (unsigned)(size_t)smem + CV_HP + (i2 + i) * CV_HS + 32, vl, vl, vl, vl);
  };
  auto x_dma = [&]() {
    const char* xs = P.ws + WS_KVT + (rb * 16 + hq) * 16384 + (hw & 1) * 8192;
    const unsigned vl = lane * 16;
#pragma unroll 1
    for (int i = 0; i < 2; ++i)
      asm volatile("s_mov_b32 m0, %0\n\ts_nop 0\n\tglobal_load_lds_dwordx4 %2, %1 sc1\n\tglobal_load_lds_dwordx4 %2, %1 offset:1024 sc1\n\t"
                   "global_load_lds_dwordx4 %2, %1 offset:2048 sc1\n\tglobal_load_lds_dwordx4 %2, %1 offset:3072 sc1"
                   ::"s"((unsigned)(size_t)smem + CV_X + (hw & 1) * 8192 + i * 4096), "s"(xs + i * 4096), "v"(vl) : "memory");
  };
  auto load_d = [&]() {
    unsigned t;
    asm volatile("s_load_dword %0, %1, 0x0\n\ts_waitcnt lgkmcnt(0)" : "=&s"(t) : "s"((const char*)P.ssm_D + (ch >> 1) * 4) : "memory");
    dw = t;
  };
  if (__builtin_expect(hw < 2, 1)) {
    const int lw = hw;
    unsigned va[4], vb[4];
    {
      const int r = lane >> 3, c = lane & 7, h = lane >> 4;
      const unsigned a0 = r * 2048 + ((c ^ h) << 4), b0 = r * 6144 + ((c ^ ((h & 1) << 1)) << 4);
#pragma unroll
      for (int j = 0; j < 4; ++j) {
        va[j] = (a0 ^ ((j & 1) << 6)) + j * (8 * 2048 - 1024);
        vb[j] = (b0 ^ ((j & 1) << 6)) + j * (8 * 6144 - 1024);
      }
    }
    const char* xa = (const char*)P.x + (rb * 128 + 64 * lw) * 2048;
    const char* wq = (const char*)P.wqkv + 32 * lw * 6144 + hq * 128;
    const char* wk = (const char*)P.wqkv + 32 * lw * 6144 + hp * 128 + 2048;
    const int ij = (idx & 3) + (idx >> 2);
    const int rot = ROTM == 0 ? 4 * (((xl & 3) + 2 * (xl >> 2) + ((idx >> 2) & 1)) & 3) : ROTM == 3 ? 8 * (ij & 1) : ROTM == 4 ? 4 * (ij & 3)
                  : ROTM == 5 ? 5 * (ij % 3) : ROTM == 6 ? 6 * (ij & 1) : ROTM == 7 ? 8 * (ij & 1) + (xl & 7) : ROTM == 8 ? 4 * (ij & 1) : ROTM == 9 ? 3 * (ij % 3) : 0;
#define LAGM 2
#define LAGN 2
    const int cls = (hp + (int)((size_t)P.wqkv >> 7)) & 7;
    const int lag = LAGM == 1 ? (cls == 3 ? LAGN : 0) : LAGM == 2 ? (cls == 3 || cls == 2 ? LAGN : 0) : LAGM == 3 ? ((idx & 3) != 0 ? LAGN : 0)
                  : LAGM == 4 ? ((idx & 3) != ((idx >> 2) & 1) ? LAGN : 0) : LAGM == 5 ? ((idx & 2) ? LAGN : 0) : 0;
    auto issue = [&](int t, int slot, auto g0_, auto g1_) {
      const int kk = (t + rot - lag) & 15;
      const unsigned sl = (unsigned)(size_t)smem + slot;
      sfor<decltype(g0_)::v, decltype(g1_)::v>([&](auto i_) {
        constexpr int ORD[3][5] = {{0, 1, 2, 3, 4}, {3, 4, 2, 0, 1}, {3, 0, 4, 1, 2}};
        constexpr int i = ORD[LORD][decltype(i_)::v];
        if constexpr (i < 2) dma4(xa + (kk * 128 + i * 32 * 2048), sl + lw * 8192 + i * 4096, va[0], va[1], va[2], va[3]);
        else dma4((i == 2 ? wq : wk) + (kk * 64 * 6144 + (i == 4 ? 2048 : 0)), sl + 16384 + (i - 2) * 8192 + lw * 4096, vb[0], vb[1], vb[2], vb[3]);
      });
    };
    TB_DECL
    issue(0, 0, IC<0>{}, IC<5>{});
    issue(1, G1_STAGE, IC<0>{}, IC<G1_K0 / 4>{});
    waitv<G1_K0>();
    TBAR(0);
    issue(1, G1_STAGE, IC<G1_K0 / 4>{}, IC<5>{});
    int slot = G1_STAGE;
#pragma unroll 1
    for (int t = 1; t < 15; ++t) {
      slot = slot + G1_STAGE == 3 * G1_STAGE ? 0 : slot + G1_STAGE;
      issue(t + 1, slot, IC<0>{}, IC<5>{});
      waitv<20>();
      TBAR(t);
    }
    if constexpr (H_AT == 10) hburst();
    {
      const unsigned st = 8 * 2048 - 1024;
      dma4((const char*)P.wo + nt * 128 + (size_t)((G2MAP ? 2 * xl + (idx >> 4) : 4 * (xl >> 1) + (idx >> 3)) * 8 + lw * 4) * 8 * 2048, (unsigned)(size_t)smem + (10 + lw * W1N) * 4096,
           voW, voW + st, voW + 2 * st, voW + 3 * st);
    }
    constexpr int NPF = 4;
    load_d();
    waitv<(H_AT == 10 ? 8 : 0) + NPF>();
    TBAR(15);
    STAMP(1);
    TB_OUT
    if constexpr (H_AT == 11) hburst();
    wo_dma(P, smem, nt, voW, lw * W1N, 2);
    if constexpr (H_AT >= 10) { waitv<8>(); lset(smem, F_WH + lw * 4, 1); }
    spin<1>([&]() { return lget(smem, F_AR) != 0; });
    wo_dma(P, smem, nt, voW, lw * W1N + 2, W1N - 2);
  } else {
    const int u = hw - 2;
    const bool leader = blockIdx.x == 0 && u == 1, follower = blockIdx.x != 0 && u == 0;
    GAS unsigned* hbp = hb_ptr(P.ws, blockIdx.x);
    unsigned h0 = 0, h1 = 0;
    if (follower) h0 = gload(hbp);
    if (u == 0) *(LDSAS u32x4*)(smem + (lane < 56 ? CV_XP + lane * 16 : CV_HP + ((lane - 56) >> 1) * CV_HS + (lane & 1) * 16)) = u32x4{0, 0, 0, 0};
    auto beat = [&]() {
      if (leader) {
#pragma unroll
        for (int i = 0; i < 4; ++i) __hip_atomic_fetch_add(hb_ptr(P.ws, i * 64 + lane), 1u, __ATOMIC_RELAXED, __HIP_MEMORY_SCOPE_AGENT);
      }
    };
    const int cg = lane & 7, rg = lane >> 3;
    const int d0 = 128 * xl + 64 * u + 8 * cg;
    const int s0 = 64 * idx + 8 * rg;
    const unsigned dump = (unsigned)(size_t)smem + X1_Z + 256 + u * 256;
    TB_DECL
    int cur = 0;
    auto advance = [&](int n) {
      sched0();
      waitl0();
#pragma unroll 1
      for (; cur < n && cur < 16; ++cur) TBAR(cur);
      sched0();
    };
#define PF2 4
    {
      const int slow = (3 - (int)(((size_t)P.wqkv >> 7) & 7) - (xl & 3) * 4) & 7;
      const int pf_n = ((idx >> 2) * 3 + ((idx & 3) - ((idx & 3) > slow ? 1 : 0))) * 76 + u * 38 + lane;
      const bool pf_on = slow < 4 && (idx & 3) != slow && lane < 38 && pf_n < 14 * 128;
      const int n2 = (pf_n % 14) * 128 + pf_n / 14;
      const unsigned pf_off = ((n2 & 127) >> 1) * 6144 + (1 + (n2 & 1)) * 2048 + ((xl & 3) * 4 + slow) * 128 + (2 + (n2 >> 7)) * 64 * 6144;
      __builtin_amdgcn_s_sleep(PF2);
      if (pf_on) touch(P.wqkv, dump, pf_off);
    }
    constexpr int A1_END = (A1_START > 3 || PF_ON == 0 ? A1_START : 3) + 22 / A1_PER + A1_LAG + (63 + A1_ROWS) / A1_ROWS;
    static_assert(A1_END + A1_STAG - 1 <= 16, "");
    static_assert(A1_END <= 16 && A1_END >= BEAT_AT + 3, "hand-shake order");
    const rsrc_t rt = mkrs(P.ws + WS_KVT, 4u << 20);
    u32x4 ld[23];
    unsigned ko = (s0 * 1024 + d0) * 2, kh = s0 == 0 ? 0x80000000u : ko - 3 * 2048;
    int vkv = (((cg >> 2) ^ QSWAP) + ((xl >> 2) * 8 + 4 * u) * 16 + (xl & 3) * 4) * 16384 + rg * 64 + (((cg ^ (2 * idx + (rg >> 2))) & 3) << 4) + idx * 512;
    asm volatile("" : "+v"(ko), "+v"(kh), "+v"(vkv));
#define SLOWG -1
    const int cls_ = (hp + (int)((size_t)P.wqkv >> 7)) & 7;
    advance(A1_START + (SLOWG >= 0 && (cls_ & 6) == 2 ? SLOWG : A1_STAG > 1 ? (idx + (xl >> 1)) % A1_STAG : 0));
    if (A1_DLY) __builtin_amdgcn_s_sleep(A1_DLY);
    STAMP(2);
    sfor<0, 23>([&](auto n_) {
      constexpr int n = decltype(n_)::v;
      if constexpr (n < 4) ld[n] = *(const u32x4*)(P.shift_k + d0 * 4 + n * 8);
      else if constexpr (n < 7) ld[n] = __builtin_amdgcn_raw_buffer_load_b128(mkrs(P.k, 4u << 20), kh + (n - 4) * 2048, 0, 0);
      else if constexpr (n < 15) ld[n] = *(const u32x4*)((const char*)P.k + (n - 7) * 2048 + ko);
      else ld[n] = *(const u32x4*)((const char*)P.v + (n - 15) * 2048 + ko);
      if constexpr (n % A1_PER == A1_PER - 1 && n != 22) {
        advance(cur + 1);
        if (A1_DLY) __builtin_amdgcn_s_sleep(A1_DLY);
      }
    });
    load_d();
    auto tburst = [&]() {
      if (lane < 32) touch(P.wo, dump, (blockIdx.x * 64 + u * 32 + lane) * 128);
    };
    if constexpr (H_AT < 0) hburst();
    if constexpr (T_AT < 0) tburst();
    advance(cur + A1_LAG);
    sfor<0, 8>([&](auto c_) {
      constexpr int c = decltype(c_)::v;
      const unsigned wa = ld[c >> 1][(c & 1) * 2], wb = ld[c >> 1][(c & 1) * 2 + 1];
      const float w0 = bflo(wa), w1 = bfhi(wa), w2 = bflo(wb), w3 = bfhi(wb);
      float kf[11], r[8];
#pragma unroll
      for (int i = 0; i < 11; ++i) {
        kf[i] = (c & 1) ? bfhi(ld[4 + i][c >> 1]) : bflo(ld[4 + i][c >> 1]);
      }
#pragma unroll
      for (int i = 0; i < 8; ++i) {
        float ks = kf[i] * w3;
        ks = __builtin_fmaf(kf[i + 1], w2, ks);
        ks = __builtin_fmaf(kf[i + 2], w1, ks);
        ks = __builtin_fmaf(kf[i + 3], w0, ks);
        r[i] = ks * ((c & 1) ? bfhi(ld[15 + i][c >> 1]) : bflo(ld[15 + i][c >> 1]));
        if ((c * 8 + i + 1) % A1_ROWS == 0 && i != 7) {
#pragma unroll
          for (int j = 0; j <= i; ++j) pin(r[j]);
          advance(cur + 1);
        }
      }
      st_sc1(u32x4{pack2(r[0], r[1]), pack2(r[2], r[3]), pack2(r[4], r[5]), pack2(r[6], r[7])}, rt, vkv, ((c >> 1) * 16 + 2 * (c & 1)) * 16384);
      if constexpr (c == 5) { if (follower) h1 = gload(hbp); }
      if constexpr (c == 7) {
        if (follower) {
          if (__builtin_amdgcn_readfirstlane(h1) == __builtin_amdgcn_readfirstlane(h0))
            spin<8>([&]() { return __builtin_amdgcn_readfirstlane(gload(hbp)) != __builtin_amdgcn_readfirstlane(h0); });
          if (lane == 0) __hip_atomic_fetch_add(ack_ptr(P.ws, xl), 1u, __ATOMIC_RELAXED, __HIP_MEMORY_SCOPE_AGENT);
        }
      }
      {
        const u32x4 mk = {MARK, MARK, MARK, MARK};
        const rsrc_t rp = mkrs(P.ws + WS_KVP, 1024 * KVP_SLOT);
        if constexpr (c == 1 || c == 3) st_sc1(mk, rp, lane * 16, ((hp * 4 + u + (c >> 1) * 2) * 16 + rb) * KVP_SLOT);
        if constexpr (c == 5) { if (lane < 16) st_sc1(mk, rp, (lane & 7) * 16 + (lane >> 3) * 32 * KVP_SLOT, ((hp * 4 + u) * 16 + rb) * KVP_SLOT + 1024); }
      }
      if constexpr (c == H_AT) hburst();
      if constexpr (c == T_AT) tburst();
      if constexpr (T_AT == 9) { if (c == (idx & 7)) tburst(); }
      if constexpr (H_AT == 9) { if (c == ((idx >> 2) & 7)) hburst(); }
      if constexpr ((c * 8 + 8) % A1_ROWS == 0 || c == 7) advance(cur + 1);
    });
    advance(cur + A_LAG);
    waitv<0>();
    gpost(P.ws, MB_A, (u * 16 + lane) * 8 + xl, 16, lane);
    if constexpr (H_AT == 8) hburst();
    if constexpr (T_AT == 8) tburst();
    STAMP(3);
    const char* abx = P.ws + WS_CNT + blockIdx.x * MB_STRIDE + MB_A * 4;
    constexpr bool xearly = false;
    if (xearly) {
      x_dma();
    }
    advance(15);
    if (!xearly && u == 0) poll_prime<0>(abx);
    advance(16);
    STAMP(1);
    TB_OUT
    if (leader) {
      GAS unsigned* ap = ack_ptr(P.ws, lane & 7);
      spin<16>([&]() {
        if (__builtin_amdgcn_ballot_w64(gload(ap) < 32u) == 0) return true;
        beat();
        return false;
      });
    }
    if (!xearly) {
      if (u == 0) poll_prime<1>(abx);
      group_wait<1, false>(smem, F_AR, u == 0, abx, 32);
      x_dma();
    } else if (u == 0) lset(smem, F_AR, 1);
    waitv<0>();
    lset(smem, F_HR + u * 4, 1);
    if constexpr (H_AT < 10) lset(smem, F_WH + u * 4, 1);
  }
  const float Dv = u2f((ch & 1) ? (dw & 0xffff0000u) : (dw << 16));
  STAMP(4);
  {
    const int l15 = lane & 15, g = lane >> 4;
    const int q4 = l15 >> 2, p4 = lane & 3;
    lptr xb[2][2];
#pragma unroll
    for (int par = 0; par < 2; ++par)
#pragma unroll
      for (int hf = 0; hf < 2; ++hf) {
        const int t4 = 28 + 16 * par + 3 + 4 * p4 - 8 * g - 4 * hf - q4;
        const int s8 = t4 >> 1;
        xb[par][hf] = smem + CV_XP + s8 * 64 + ((hw ^ (((s8 + 2) >> 2) & 3)) << 4) + (t4 & 1) * 8;
      }
    lptr hb = smem + CV_HP + hw * CV_HS + 32 - 24 + 8 * p4 + 64 * g + 8 * q4;
    const rsrc_t ry = mkrs(P.ws + WS_Y2T, 4u << 20);
    int vy = ch * 4096 + lane * 16;
    asm volatile("" : "+v"(xb[0][0]), "+v"(xb[0][1]), "+v"(xb[1][0]), "+v"(xb[1][1]), "+v"(hb), "+v"(vy));
    __builtin_amdgcn_s_setprio(H_PRIO);
    spin<1>([&]() { return lmin2(smem, F_HR) != 0 && lmin2(smem, F_WH) != 0; });
    if (lane == 0) {
      volatile LDSAS u16* h0 = (volatile LDSAS u16*)(smem + CV_HP + hw * CV_HS + 32);
      *h0 = (u16)pack2(bflo(*h0) + Dv, 0.f);
    }
    const char* wsrc = (const char*)P.wo + nt * 128 + (size_t)((hw >> 1) * 80 + (hw & 1) * 20) * 8 * 2048;
    const unsigned wdst = (unsigned)(size_t)smem + ((hw >> 1) * 80 + (hw & 1) * 20) * 1024;
    const int gq = 8 * idx + (lane >> 3);
    unsigned qo = (((gq & 63) + 512 * (gq >> 6) + 256 * (hw >> 1)) * 1024 + 128 * xl + 64 * (hw & 1) + 8 * (lane & 7)) * 2;
    unsigned fb = (unsigned)(size_t)smem + L_FLG + hw * 4;
    asm volatile("" : "+v"(qo), "+v"(fb));
    spin<1>([&]() { return lmin4(smem, GATE) != 0; });
    if (hw < 2) { waitv<0>(); lset(smem, F_HR + 8 + hw * 4, 1); }
    STAMP(5);
    asm volatile(CONV_ASM
                 : "=&v"(qv[0]), "=&v"(qv[1]), "=&v"(qv[2]), "=&v"(qv[3])
                 : "v"(xb[0][0]), "v"(xb[0][1]), "v"(xb[1][0]), "v"(xb[1][1]), "v"(hb), "v"(vy), "v"(voW), "v"(qo), "v"(fb),
                   "s"(ry), "s"(wsrc), "s"(wdst), "s"(P.q)
                 : "memory", "vcc", "scc", "s40", "s41", "s42", CONV_CLOBBERS);
    STAMP(6);
  }
  {
    const int u = hw & 1, jh = hw >> 1;
    const int cg = lane & 7, rg = lane >> 3;
    const int gq = 8 * idx + rg;
    const int sq = (gq & 63) + 512 * (gq >> 6) + 256 * jh;
    spin<1>([&]() { return lmin4(smem, F_CVD) != 0; });
#pragma unroll
    for (int i = 0; i < 2; ++i)
      dma16s((const char*)P.wo + nt * 128 + (AH(30) * 4 + hw * 2 + i) * 16384, (unsigned)(size_t)smem + (120 + hw * 2 + i) * 1024, voW);
    waitv<0>();
    lset(smem, F_WL + hw * 4, 1);
    gsignal4(P.ws, smem, F_NY, MB_Y, lane * 8 + xl, 32, lane);
    STAMP(7);
    __builtin_amdgcn_s_setprio(A3_PRIO);
    float qf[4][8], yf[4][8];
    asm volatile("" : "+v"(qv[0]), "+v"(qv[1]), "+v"(qv[2]), "+v"(qv[3]));
#pragma unroll
    for (int j = 0; j < 4; ++j)
#pragma unroll
      for (int c = 0; c < 8; ++c) { qf[j][c] = (c & 1) ? bfhi(qv[j][c >> 1]) : bflo(qv[j][c >> 1]); yf[j][c] = 0.f; pin(qf[j][c]); pin(yf[j][c]); }
    const char* yb = P.ws + WS_Y2T + (128 * xl + 32 * hw) * 4096 + idx * 128;
    unsigned vy[4];
#pragma unroll
    for (int i = 0; i < 4; ++i) vy[i] = rg * 4096 + ((cg ^ ((4 * hw + i) & 7)) << 4) + i * (8 * 4096 - 1024);
    const unsigned yl = (unsigned)(size_t)smem + L_YT + hw * 4096;
    unsigned tr = (unsigned)(size_t)smem + L_YT + (64 * u + 8 * cg) * 128 + ((rg ^ cg) << 4) + jh * 8;
    unsigned oo = (sq * 2048 + 128 * xl + 64 * u + 8 * cg) * 2;
    asm volatile("" : "+s"(yb), "+v"(vy[0]), "+v"(vy[1]), "+v"(vy[2]), "+v"(vy[3]), "+v"(tr), "+v"(oo));
    group_wait<2, true>(smem, F_YR, hw == 0, P.ws + WS_CNT + blockIdx.x * MB_STRIDE + MB_Y * 4, 32);
    STAMP(8);
    asm volatile("s_mov_b32 m0, %0\n\ts_nop 0\n\tglobal_load_lds_dwordx4 %2, %1 sc1\n\tglobal_load_lds_dwordx4 %3, %1 offset:1024 sc1\n\t"
                 "global_load_lds_dwordx4 %4, %1 offset:2048 sc1\n\tglobal_load_lds_dwordx4 %5, %1 offset:3072 sc1\n\ts_waitcnt vmcnt(0)"
                 ::"s"(yl), "s"(yb), "v"(vy[0]), "v"(vy[1]), "v"(vy[2]), "v"(vy[3]) : "memory");
    lset(smem, F_YL + hw * 4, 1);
    spin<1>([&]() { return lmin2(smem, F_YL + u * 8) != 0; });
    sfor<0, 4>([&](auto j_) {
      constexpr int j = decltype(j_)::v;
      float* y = yf[j];
      const unsigned a = tr;
      asm volatile("ds_read_u16_d16_hi %0, %8 offset:%9\n\tds_read_u16_d16_hi %1, %8 offset:%9+128\n\tds_read_u16_d16_hi %2, %8 offset:%9+256\n\t"
                   "ds_read_u16_d16_hi %3, %8 offset:%9+384\n\tds_read_u16_d16_hi %4, %8 offset:%9+512\n\tds_read_u16_d16_hi %5, %8 offset:%9+640\n\t"
                   "ds_read_u16_d16_hi %6, %8 offset:%9+768\n\tds_read_u16_d16_hi %7, %8 offset:%9+896"
                   : "+v"(y[0]), "+v"(y[1]), "+v"(y[2]), "+v"(y[3]), "+v"(y[4]), "+v"(y[5]), "+v"(y[6]), "+v"(y[7]) : "v"(a), "i"(2 * j) : "memory");
      if constexpr (j > 0) {
        float* z = yf[j - 1];
        asm volatile("s_waitcnt lgkmcnt(8)" : "+v"(z[0]), "+v"(z[1]), "+v"(z[2]), "+v"(z[3]), "+v"(z[4]), "+v"(z[5]), "+v"(z[6]), "+v"(z[7])::"memory");
        st_out<OUTAUX3>(u32x4{pack2(qf[j - 1][0] * z[0], qf[j - 1][1] * z[1]), pack2(qf[j - 1][2] * z[2], qf[j - 1][3] * z[3]),
                              pack2(qf[j - 1][4] * z[4], qf[j - 1][5] * z[5]), pack2(qf[j - 1][6] * z[6], qf[j - 1][7] * z[7])}, P.out, oo + (j - 1) * 64 * 4096);
        sched0();
      }
    });
    {
      float* z = yf[3];
      asm volatile("s_waitcnt lgkmcnt(0)" : "+v"(z[0]), "+v"(z[1]), "+v"(z[2]), "+v"(z[3]), "+v"(z[4]), "+v"(z[5]), "+v"(z[6]), "+v"(z[7])::"memory");
      st_out<OUTAUX3>(u32x4{pack2(qf[3][0] * z[0], qf[3][1] * z[1]), pack2(qf[3][2] * z[2], qf[3][3] * z[3]),
                            pack2(qf[3][4] * z[4], qf[3][5] * z[5]), pack2(qf[3][6] * z[6], qf[3][7] * z[7])}, P.out, oo + 3 * 64 * 4096);
    }
  }
  STAMP(9);
  ST_OUT;
  PAD_END;
}

__global__ __launch_bounds__(512, 2) void fused_kernel(Params P) {
  extern __shared__ __attribute__((aligned(16))) char smem_[];
  lptr smem = (lptr)smem_;
  const void* pc0;
  asm volatile("s_getpc_b64 %0" : "=s"(pc0));
  const int lane = threadIdx.x & 63, wid = __builtin_amdgcn_readfirstlane(threadIdx.x >> 6);
  const int xl = blockIdx.x & 7, idx = blockIdx.x >> 3;
  if (__builtin_expect(wid >= 4, 1)) helper_wave(P, smem, wid - 4, lane, xl, idx);
  else compute_wave(P, smem, wid, lane, xl, idx, pc0);
}

#if defined(__HIP_DEVICE_COMPILE__)
#pragma clang attribute pop
#endif

__global__ void tiny3_k(char* ws) { unsigned long long ta_ = *(volatile unsigned long long*)(ws + (30u << 20)); while (__builtin_amdgcn_s_memrealtime() - ta_ < 6000) __builtin_amdgcn_s_sleep(4); }
extern "C" void kernel_launch(void* const* d_in, const int* in_sizes, int n_in,
                              void* d_out, int out_size, void* d_ws, size_t ws_size,
                              hipStream_t stream) {
  Params P;
  P.q = (const u16*)d_in[0];
  P.k = (const u16*)d_in[1];
  P.v = (const u16*)d_in[2];
  P.shift_k = (const u16*)d_in[3];
  P.ssm_k = (const u16*)d_in[4];
  P.ssm_D = (const u16*)d_in[5];
  P.x = (const u16*)d_in[6];
  P.wqkv = (const u16*)d_in[7];
  P.wo = (const u16*)d_in[8];
  P.ws = (char*)d_ws;
  P.out = (u16*)d_out;
  (void)hipFuncSetAttribute((const void*)fused_kernel, hipFuncAttributeMaxDynamicSharedMemorySize, SMEM_BYTES);
  fused_kernel<<<256, 512, SMEM_BYTES, stream>>>(P);
  tiny3_k<<<1, 64, 0, stream>>>(P.ws);
}
```

```cpp
#include <hip/hip_runtime.h>
#include <stdint.h>

typedef __attribute__((ext_vector_type(8))) __bf16 bf16x8;
typedef __attribute__((ext_vector_type(4))) __bf16 bf16x4;
typedef __attribute__((ext_vector_type(2))) __bf16 bf16x2;
typedef __attribute__((ext_vector_type(4))) float f32x4;
typedef __attribute__((ext_vector_type(2))) float f32x2;
typedef __attribute__((ext_vector_type(4))) unsigned int u32x4;
typedef __attribute__((ext_vector_type(2))) unsigned int u32x2;
typedef unsigned short u16;
#define LDSAS __attribute__((address_space(3)))
#define GAS __attribute__((address_space(1)))
typedef LDSAS char* lptr;
typedef __amdgpu_buffer_rsrc_t rsrc_t;

#define WS_KVT   (0u)
#define WS_Y2T   (8u << 20)
#define WS_KVP   (12u << 20)
#define KVP_SLOT 1152
#define WS_CNT   (13u << 20 | 512u << 10)
#define WS_USED  (14u << 20)
#define WS_STAMP (20u << 20)
#define WS_SPAN (14u << 20)
#define MB_P 0
#define MB_O 1
#define MB_O2 2
#define MB_A 4096
#define MB_Y 4097
#define MB_STRIDE 64
#define MARK 0xFFBFFFBFu

#define G1_STAGE 40960
#define CV_XP 122880
#define CV_X (CV_XP + 896)
#define CV_HP (CV_X + 16384)
#define CV_HS 4128
#define L_SCR 159360
#define L_FLG 163456
#define L_YT 132096
#define F_X1 (L_FLG + 0)
#define F_WC (L_FLG + 144)
#define F_YL (L_FLG + 160)
#define F_T15 (L_FLG + 128)
#define F_X2 (L_FLG + 16)
#define F_HR (L_FLG + 32)
#define F_CVD (L_FLG + 48)
#define F_WH (L_FLG + 64)
#define F_WL (L_FLG + 80)
#define F_NP (L_FLG + 96)
#define F_NO (L_FLG + 100)
#define F_NY (L_FLG + 104)
#define F_PR (L_FLG + 108)
#define F_OR (L_FLG + 112)
#define F_YR (L_FLG + 116)
#define F_AR (L_FLG + 120)
#define SMEM_BYTES 163840
#define X1_Z 156672

#define CODE_BYTES 32000
#define SPIN_LIMIT (1 << 17)

#define ST_DECL
#define ST_OUT
#define STAMP(slot) do {} while (0)
#define CNT_SLOW(k)
#define PAD_DECL(role)
#define PAD_END
#define TB_DECL
#define TBAR(t) bar()
#define TB_OUT
struct Params {
  const u16 *q, *k, *v, *shift_k, *ssm_k, *ssm_D, *x, *wqkv, *wo;
  char* ws;
  u16* out;
};

#if defined(__HIP_DEVICE_COMPILE__)
#pragma clang attribute push(__attribute__((target("no-packed-fp32-ops"))), apply_to = function)
#endif
__device__ __forceinline__ float u2f(unsigned x) { return __builtin_bit_cast(float, x); }
__device__ __forceinline__ float i2f(int x) { return __builtin_bit_cast(float, x); }
__device__ __forceinline__ int f2i(float x) { return __builtin_bit_cast(int, x); }
template <int N> struct IC { static constexpr int v = N; };
template <int B, int E, class F> __device__ __forceinline__ void sfor(F f) {
  if constexpr (B < E) { f(IC<B>{}); sfor<B + 1, E>(f); }
}
template <int N> __device__ __forceinline__ void waitv() { asm volatile("s_waitcnt vmcnt(%0)" ::"i"(N) : "memory"); }
__device__ __forceinline__ void waitl0() { asm volatile("s_waitcnt lgkmcnt(0)" ::: "memory"); }
__device__ __forceinline__ void wait_all() { __builtin_amdgcn_s_waitcnt(0x0070); asm volatile("s_waitcnt vmcnt(0) lgkmcnt(0)" ::: "memory"); }
__device__ __forceinline__ void bar() { asm volatile("s_barrier" ::: "memory"); }
__device__ __forceinline__ void sched0() { __builtin_amdgcn_sched_barrier(0); }
__device__ __forceinline__ void cfence() { asm volatile("" ::: "memory"); }

__device__ __forceinline__ rsrc_t mkrs(const void* p, unsigned bytes) {
  return __builtin_amdgcn_make_buffer_rsrc((void*)p, 0, bytes, 0x00020000);
}
__device__ __forceinline__ void dma16(const void* base, lptr dst, unsigned voff, unsigned soff) {
  __builtin_amdgcn_global_load_lds((const GAS void*)((const char*)base + (size_t)soff + voff), (LDSAS void*)dst, 16, 0, 0);
}
__device__ __forceinline__ void dma16s(const void* sbase, unsigned lds_addr, unsigned voff) {
  asm volatile("s_mov_b32 m0, %0\n\ts_nop 0\n\tglobal_load_lds_dwordx4 %1, %2" ::"s"(lds_addr), "v"(voff), "s"(sbase) : "memory");
}
__device__ __forceinline__ void dma4(const void* sbase, unsigned lds_addr, unsigned v0, unsigned v1, unsigned v2, unsigned v3) {
  asm volatile("s_mov_b32 m0, %0\n\ts_nop 0\n\tglobal_load_lds_dwordx4 %2, %1\n\tglobal_load_lds_dwordx4 %3, %1 offset:1024\n\t"
               "global_load_lds_dwordx4 %4, %1 offset:2048\n\tglobal_load_lds_dwordx4 %5, %1 offset:3072"
               ::"s"(lds_addr), "s"(sbase), "v"(v0), "v"(v1), "v"(v2), "v"(v3) : "memory");
}
__device__ __forceinline__ void touch(const void* sbase, unsigned lds_addr, unsigned voff) {
  asm volatile("s_mov_b32 m0, %0\n\ts_nop 0\n\tglobal_load_lds_dword %2, %1" ::"s"(lds_addr), "s"(sbase), "v"(voff) : "memory");
}
__device__ __forceinline__ u32x4 ld_sc1(rsrc_t rs, int voff, int soff) { return __builtin_amdgcn_raw_buffer_load_b128(rs, voff, soff, 16); }
__device__ __forceinline__ void st_sc1(u32x4 v, rsrc_t rs, int voff, int soff) { __builtin_amdgcn_raw_buffer_store_b128(v, rs, voff + soff, 0, 16); }
#define OUTAUX 16
#define OUTAUX3 OUTAUX
template <int AUX> __device__ __forceinline__ void st_out(u32x4 v, void* out, unsigned off) { __builtin_amdgcn_raw_buffer_store_b128(v, mkrs(out, 8u << 20), off, 0, AUX); }
__device__ __forceinline__ float dot2(unsigned a, unsigned b, float c) {
  return __builtin_amdgcn_fdot2_f32_bf16(__builtin_bit_cast(bf16x2, a), __builtin_bit_cast(bf16x2, b), c, false);
}
__device__ __forceinline__ void pin(float& x) { asm volatile("" : "+v"(x)); }
__device__ __forceinline__ unsigned pack2(float lo, float hi) {
  f32x2 v = {lo, hi};
  return __builtin_bit_cast(unsigned, __builtin_convertvector(v, bf16x2));
}
__device__ __forceinline__ float bflo(unsigned w) { return u2f(w << 16); }
__device__ __forceinline__ float bfhi(unsigned w) { return u2f(w & 0xffff0000u); }
__device__ __forceinline__ float phi_f(float t1) {
  const float e = __builtin_amdgcn_exp2f(__builtin_fmaf(t1, 1.44269504088896f, -1.44269504088896f));
  return __builtin_amdgcn_fmed3f(e, t1, 1.f) + 1e-6f;
}
__device__ __forceinline__ bf16x8 trread2(lptr p0, lptr p1) {
  bf16x4 lo = __builtin_amdgcn_ds_read_tr16_b64_v4bf16((LDSAS bf16x4*)p0);
  bf16x4 hi = __builtin_amdgcn_ds_read_tr16_b64_v4bf16((LDSAS bf16x4*)p1);
  return __builtin_shufflevector(lo, hi, 0, 1, 2, 3, 4, 5, 6, 7);
}
#define MFMA16(a, b, c) __builtin_amdgcn_mfma_f32_16x16x32_bf16((a), (b), (c), 0, 0, 0)
#define BC8(x) __builtin_bit_cast(bf16x8, (x))
__device__ __forceinline__ float shr1(float v) {
  return i2f(__builtin_amdgcn_update_dpp(0, f2i(v), 0x138, 0xf, 0xf, true));
}
__device__ __forceinline__ float fsel(bool c, float a, float b) { return c ? a : b; }
__device__ __forceinline__ float ror1(float v) {
  return i2f(__builtin_amdgcn_update_dpp(0, f2i(v), 0x13C, 0xf, 0xf, true));
}
template <int S> __device__ __forceinline__ float row_ror(float v) {
  return i2f(__builtin_amdgcn_update_dpp(0, f2i(v), 0x120 + S, 0xf, 0xf, true));
}

__device__ __forceinline__ void lset(lptr smem, int off, int val) {
  asm volatile("s_mov_b64 exec, 1\n\tds_write_b32 %0, %1\n\ts_mov_b64 exec, -1" ::"v"((unsigned)(size_t)smem + off), "v"(val) : "memory");
}
__device__ __forceinline__ int lmin2(lptr smem, int off) {
  const u32x2 v = *(volatile LDSAS u32x2*)(smem + off);
  return __builtin_amdgcn_readfirstlane((int)(v[0] < v[1] ? v[0] : v[1]));
}
__device__ __forceinline__ int lmin4(lptr smem, int off) {
  const u32x4 v = *(volatile LDSAS u32x4*)(smem + off);
  const unsigned a = v[0] < v[1] ? v[0] : v[1], b = v[2] < v[3] ? v[2] : v[3];
  return __builtin_amdgcn_readfirstlane((int)(a < b ? a : b));
}
__device__ __forceinline__ int lget(lptr smem, int off) {
  return __builtin_amdgcn_readfirstlane(*(volatile LDSAS int*)(smem + off));
}
template <int SLEEP, class F> __device__ __forceinline__ void spin(F ready) {
#pragma unroll 1
  for (int it = 0; it < SPIN_LIMIT; ++it) {
    if (ready()) break;
    if (SLEEP) __builtin_amdgcn_s_sleep(SLEEP);
  }
  cfence();
}
__device__ __forceinline__ GAS unsigned* cnt_ptr(char* ws, int wg) { return (GAS unsigned*)(ws + WS_CNT + wg * MB_STRIDE); }
__device__ __forceinline__ GAS unsigned* ack_ptr(char* ws, int i) { return (GAS unsigned*)(ws + WS_CNT + 32768 + i * 128); }
__device__ __forceinline__ GAS unsigned* hb_ptr(char* ws, int wg) { return (GAS unsigned*)(ws + WS_CNT + 32768 + 1024 + wg * 4); }
__device__ __forceinline__ unsigned gload(GAS unsigned* p) { return __hip_atomic_load(p, __ATOMIC_RELAXED, __HIP_MEMORY_SCOPE_AGENT); }
__device__ __forceinline__ void gpost(char* ws, int kind, int wg, int n, int lane) {
  if (lane < n) __hip_atomic_fetch_add(cnt_ptr(ws, wg) + kind, 1u, __ATOMIC_RELAXED, __HIP_MEMORY_SCOPE_AGENT);
}
__device__ __forceinline__ void gsignal4(char* ws, lptr smem, int cnt, int kind, int wg, int n, int lane) {
  unsigned old;
  asm volatile("s_mov_b64 exec, 1\n\tds_add_rtn_u32 %0, %1, %2\n\ts_mov_b64 exec, -1\n\ts_waitcnt lgkmcnt(0)"
               : "=&v"(old) : "v"((unsigned)(size_t)smem + cnt), "v"(1u) : "memory");
  if (__builtin_amdgcn_readfirstlane(old) == 3) gpost(ws, kind, wg, n, lane);
}
#define PSLEEP 4
__device__ __forceinline__ void gwait(char* ws, int kind, unsigned target) {
  GAS unsigned* p = cnt_ptr(ws, blockIdx.x) + kind;
  spin<PSLEEP>([&]() { return __builtin_amdgcn_readfirstlane(gload(p)) >= target; });
}

#define LDG(dst, voff, base, off) asm volatile("global_load_dwordx4 %0, %1, %2 offset:" #off " sc1" : "=v"(dst) : "v"(voff), "s"(base) : "memory")

struct G1Frag { bf16x8 a[4], b[6]; };

template <int N> __device__ __forceinline__ void post(const char* base, unsigned voff) {
  asm volatile("s_mov_b64 exec, %3\n\tglobal_atomic_add %0, %1, %2\n\ts_mov_b64 exec, -1" ::"v"(voff), "v"(1u), "s"(base), "i"(N == 16 ? 0xffffll : 0xffffffffll) : "memory");
}
__device__ __forceinline__ unsigned arrive(lptr smem, int flag, int cnt) {
  unsigned old;
  asm volatile("s_mov_b64 exec, 1\n\tds_write_b32 %1, %3\n\tds_add_rtn_u32 %0, %2, %3\n\ts_mov_b64 exec, -1"
               : "=&v"(old) : "v"((unsigned)(size_t)smem + flag), "v"((unsigned)(size_t)smem + cnt), "v"(1u) : "memory");
  return old;
}
__device__ __forceinline__ bool was_last(unsigned old) {
  asm volatile("s_waitcnt lgkmcnt(0)" : "+v"(old)::"memory");
  return __builtin_amdgcn_readfirstlane(old) == 3;
}
#define PSL 3
#define POLL1(NEW, OLD) "global_atomic_add " NEW ", v252, v252, %2 sc0\n\ts_waitcnt vmcnt(2)\n\tv_readfirstlane_b32 %1, " OLD "\n\t" \
                        "s_cmp_ge_u32 %1, %3\n\ts_cbranch_scc1 2f\n\ts_sleep %5\n\t"
template <int R> __device__ __forceinline__ void poll_prime(const char* base) {
  if constexpr (R == 0)
    asm volatile("s_mov_b64 exec, 1\n\tv_mov_b32 v252, 0\n\tglobal_atomic_add v253, v252, v252, %0 sc0\n\ts_mov_b64 exec, -1" ::"s"(base) : "memory", "v252", "v253");
  else
    asm volatile("s_mov_b64 exec, 1\n\tv_mov_b32 v252, 0\n\tglobal_atomic_add v254, v252, v252, %0 sc0\n\ts_mov_b64 exec, -1" ::"s"(base) : "memory", "v252", "v254");
}
__device__ __forceinline__ void poll_loop(lptr smem, int flag, const char* base, unsigned target) {
  int it = SPIN_LIMIT;
  unsigned t;
  asm volatile("s_mov_b64 exec, 1\n\tv_mov_b32 v252, 0\n"
               "1:\n\t" POLL1("v255", "v253") POLL1("v253", "v254") POLL1("v254", "v255")
               "s_sub_u32 %0, %0, 1\n\ts_cbranch_scc0 1b\n"
               "2:\n\tv_mov_b32 v252, 1\n\tds_write_b32 %4, v252\n\ts_mov_b64 exec, -1"
               : "+s"(it), "=&s"(t) : "s"(base), "s"(target), "v"((unsigned)(size_t)smem + flag), "i"(PSL)
               : "memory", "scc", "v252", "v253", "v254", "v255");
}
template <int SLEEP, bool PRIME> __device__ __forceinline__ void group_wait(lptr smem, int flag, bool poller, const char* base, unsigned target) {
  if (poller) {
    if (PRIME) { poll_prime<0>(base); __builtin_amdgcn_s_sleep(PSL); poll_prime<1>(base); __builtin_amdgcn_s_sleep(PSL); }
    poll_loop(smem, flag, base, target);
  } else spin<SLEEP>([&]() { return lget(smem, flag) != 0; });
}
#define LDG2(dst, voff, base, off) asm volatile("global_load_dwordx2 %0, %1, %2 offset:" #off " sc1" : "=v"(dst) : "v"(voff), "s"(base) : "memory")
template <int OFF> __device__ __forceinline__ void ldg_sc1(u32x4& dst, unsigned voff, const char* base) {
  asm volatile("global_load_dwordx4 %0, %1, %2 offset:%3 sc1" : "=v"(dst) : "v"(voff), "s"(base), "i"(OFF) : "memory");
}
__device__ __forceinline__ bool any_lane(bool c) { return __builtin_amdgcn_ballot_w64(c) != 0; }
__device__ __forceinline__ bool has_mark(u32x4 v) { return (v[0] == MARK) | (v[1] == MARK) | (v[2] == MARK) | (v[3] == MARK); }
__device__ __forceinline__ unsigned wo_voff(int lane) { return (lane >> 3) * 2048 + (((lane & 7) ^ (((lane >> 4) & 3) << 1)) << 4); }
#define HD(h) ((h) < 30 ? ((h) + 10) % 30 : (h))
#define AH(h) ((int)((0x51764320u >> (4 * ((h) >> 2))) & 15u) * 4 + ((h) & 3))
__device__ __forceinline__ void wo_dma(const Params& P, lptr smem, int nt, unsigned voW, int h0, int n) {
  const unsigned st = 8 * 2048 - 1024;
#pragma unroll 1
  for (int h = h0; h < h0 + n; ++h)
    dma4((const char*)P.wo + nt * 128 + AH(h) * 65536, (unsigned)(size_t)smem + (h < 20 ? h + 10 : h - 20) * 4096, voW, voW + st, voW + 2 * st, voW + 3 * st);
}
#define MK_MODE 0
#define MK_DELAY 10
#define ZERO_DELAY 16
#define BEAT_AT 3
#define G2MAP 0
#define NTX 2
#define QSWAP 1
#define OD 16
#define P_POST 1
#define O_POST 2
#define O2_AT (24 - OD / 2 - (OD >= 16 ? 6 : OD / 2 - 1))
#define PQ_PRE 2
#define W2_POLLER 0
#define W3_EARLY 0
#define SNAKE 1
#define W1N 5
#define W1_GATE 1
#define CSL 0
#define A3_PRIO 1
#define CHK_TO 12
#define G2_PRIO 1
#define H_PRIO 3
#define C_PRIO 3

__device__ __forceinline__ void compute_wave(const Params& P, lptr smem, int wid, int lane, int xl, int idx, const void* pc0) {
  const int l15 = lane & 15, g = lane >> 4;
  const int q4 = l15 >> 2, p4 = lane & 3;
  const int hp = (xl & 3) * 4 + (idx & 3), rb = (xl >> 2) * 8 + (idx >> 2);
  const int hq = hp ^ QSWAP;
  const int nt = G2MAP ? ((idx & 15) ^ NTX) : 8 * (xl & 1) + ((idx & 7) ^ NTX);
  const int wr = wid >> 1, wh = wid & 1;
  PAD_DECL(0)
  constexpr int LAST_ = 10;
  ST_DECL
  STAMP(0);
  if (wid == 0) *(LDSAS int*)(smem + L_FLG + lane * 4) = 0;
  if (wid == 1 && lane < 9 && (idx * 9 + lane) * 128 < CODE_BYTES) touch(pc0, (unsigned)(size_t)smem + L_SCR + 512, (idx * 9 + lane) * 128);
  const char* cntb = P.ws + WS_CNT;
  const char* mbx = cntb + blockIdx.x * MB_STRIDE;
  if (blockIdx.x == 0) {
    const rsrc_t rc = mkrs(cntb, 32768 + 1024);
    __builtin_amdgcn_s_sleep(ZERO_DELAY);
#pragma unroll 1
    for (int i = 0; i < 8; ++i) st_sc1(u32x4{0u, 0u, 0u, 0u}, rc, lane * 16, (wid * 8 + i) * 1024);
    if (wid == 3) st_sc1(u32x4{lane == 0 ? 1u : 0u, 0u, 0u, 0u}, rc, lane * 16, 32768);
  }
  u32x4 pqf[4], pqs[4], whv;
  float pq[4][8];
  f32x4 s0, s1;
  bool oready = false;
  int vst = lane * 16 + (rb * 16 + hq) * 16384 + (wr * 4 + wh) * 2048;
  int vst2 = vst + 4096;
  unsigned vpostO = (G2MAP ? (16 * (rb & 1) + (lane & 15)) * 8 + (rb >> 1) : (8 * (rb & 3) + (lane & 7)) * 8 + 2 * (rb >> 2) + ((lane >> 3) & 1)) * MB_STRIDE;
  lptr xp = smem + L_SCR + (wid ^ 2) * 1024 + lane * 16;
  lptr k0 = smem + L_SCR + (wh + 2) * 1024 + lane * 16 + wr * 8, k1 = smem + L_SCR + wh * 1024 + lane * 16 + (wr ^ 1) * 8;
  lptr k0x = (lptr)((unsigned)(size_t)k0 ^ 8u), k1x = (lptr)((unsigned)(size_t)k1 ^ 8u);
  const rsrc_t ro = mkrs(P.ws + WS_KVT, 4u << 20);
  asm volatile("" : "+v"(vst), "+v"(vst2), "+v"(vpostO), "+v"(xp), "+v"(k0), "+v"(k1), "+v"(k0x), "+v"(k1x));
  unsigned voW = wo_voff(lane);
  asm volatile("" : "+v"(voW));
  {
    f32x4 acc[6][4];
#pragma unroll
    for (int i = 0; i < 6; ++i)
#pragma unroll
      for (int j = 0; j < 4; ++j) { const float c0 = i < 4 ? 1.f : 0.f; acc[i][j] = f32x4{c0, c0, c0, c0}; asm volatile("" : "+v"(acc[i][j])); }
    const int abase = (64 * wr + l15) * 128 + ((g ^ (l15 >> 1)) << 4);
    const int bbase = (16384 + (8 * g + q4) * 128 + (((4 * wh + (p4 >> 1)) ^ (((q4 >> 1) | ((g & 1) << 1)) << 1)) << 4) + (p4 & 1) * 8) ^ (wr << 5);
    lptr pa0 = smem + abase, pa1 = smem + (abase ^ 64), pb0 = smem + bbase, pb1 = smem + (bbase ^ 32);
    auto ld = [&](G1Frag& f, int ks) {
      lptr pa = ks ? pa1 : pa0;
      lptr q0 = pb0 + ks * 4096, q1 = pb1 + ks * 4096;
      f.b[0] = trread2(q0, q0 + 512);
      f.a[0] = *(LDSAS bf16x8*)(pa);
      f.a[1] = *(LDSAS bf16x8*)(pa + 2048);
      f.a[2] = *(LDSAS bf16x8*)(pa + 4096);
      f.a[3] = *(LDSAS bf16x8*)(pa + 6144);
      f.b[1] = trread2(q1, q1 + 512);
      f.b[2] = trread2(q0 + 8192, q0 + 8192 + 512);
      f.b[3] = trread2(q1 + 8192, q1 + 8192 + 512);
      f.b[4] = trread2(q0 + 16384, q0 + 16384 + 512);
      f.b[5] = trread2(q1 + 16384, q1 + 16384 + 512);
    };
    auto mm1 = [&](const G1Frag& f, int i, int j) {
      acc[i][j] = i < 2 ? MFMA16(f.b[i], f.a[j], acc[i][j]) : MFMA16(f.a[j], f.b[i], acc[i][j]);
    };
    auto mm = [&](const G1Frag& f, int i0, int i1) {
#pragma unroll
      for (int i = i0; i < i1; ++i)
#pragma unroll
        for (int j = 0; j < 4; ++j) mm1(f, i, SNAKE && (i & 1) ? 3 - j : j);
    };
    auto ilv = [&]() {
#pragma unroll
      for (int i = 0; i < 16; ++i) {
        __builtin_amdgcn_sched_group_barrier(0x008, 1, 0);
        __builtin_amdgcn_sched_group_barrier(0x100, 1, 0);
      }
      sched0();
    };
    G1Frag F0, F1;
    u32x4 ones = {0x3f803f80u, 0x3f803f80u, 0x3f803f80u, 0x3f803f80u};
    lptr x1w = smem + L_SCR + wid * 1024 + lane * 16, x1r = smem + L_SCR + (wid ^ 2) * 1024 + lane * 16, x1zr = smem + X1_Z + (wid ^ 2) * 128 + g * 16;
    lptr x1zw = smem + X1_Z + wid * 128 + g * 16 + (l15 & 1) * 64;
    const rsrc_t rp = mkrs(P.ws + WS_KVP, 1024 * KVP_SLOT);
    int vkvp = lane * 16 + (((hp * 2 + wh) * 2 + wr) * 16 + rb) * KVP_SLOT;
    int vzp = g * 16 + (l15 & 1) * 64 + 1024 + (((hp * 2 + wh) * 2 + wr) * 16 + rb) * KVP_SLOT;
    unsigned vpostP = (((lane & 7) * 4 + (hq & 3)) * 8 + ((lane >> 3) & 1) * 4 + (hq >> 2)) * MB_STRIDE;
    asm volatile("" : "+v"(ones), "+v"(x1w), "+v"(x1r), "+v"(x1zr), "+v"(x1zw), "+v"(vkvp), "+v"(vzp), "+v"(vpostP));
    __builtin_amdgcn_s_waitcnt(0xC07F);
    sched0();
    bar();
    STAMP(1);
    __builtin_amdgcn_s_waitcnt(0xC07F);
    ld(F0, 0);
    sched0();
    int ph = 0, delta = G1_STAGE;
    int beat_at = blockIdx.x == 0 ? (wid == 3 ? BEAT_AT : BEAT_AT - 1) : -1;
    asm volatile("" : "+s"(beat_at));
#pragma unroll 1
    for (int t = 0; t < 15; ++t) {
      ld(F1, 1); mm(F0, 0, 4); ilv();
      mm1(F0, 4, 0); sched0();
      mm1(F0, 4, 1); sched0();
      pa0 += delta; pb0 += delta; asm volatile("" : "+v"(pa0), "+v"(pb0)); sched0();
      mm1(F0, 4, 2); sched0();
      pa1 += delta; pb1 += delta; asm volatile("" : "+v"(pa1), "+v"(pb1)); sched0();
      mm1(F0, 4, 3); mm1(F0, 5, SNAKE ? 3 : 0); mm1(F0, 5, SNAKE ? 2 : 1); mm1(F0, 5, SNAKE ? 1 : 2);
      sched0();
      __builtin_amdgcn_s_waitcnt(0xC07F);
      mm1(F0, 5, SNAKE ? 0 : 3);
      sched0();
      bar();
      sched0();
      ld(F0, 0); mm(F1, 0, 4); ilv();
      mm1(F1, 4, 0); sched0();
      ph = ph == 2 ? 0 : ph + 1;
      asm volatile("" : "+s"(ph)); sched0();
      mm1(F1, 4, 1); sched0();
      delta = ph == 2 ? -2 * G1_STAGE : G1_STAGE;
      asm volatile("" : "+s"(delta)); sched0();
      mm1(F1, 4, 2); sched0();
      if (__builtin_expect(t == beat_at, 0)) {
        waitv<0>();
#pragma unroll 1
        for (int i = 0; i < (wid == 3 ? 4 : 0); ++i) __hip_atomic_fetch_add(hb_ptr(P.ws, i * 64 + lane), 1u, __ATOMIC_RELAXED, __HIP_MEMORY_SCOPE_AGENT);
      }
      sched0();
      mm1(F1, 4, 3);
      mm(F1, 5, 6);
      sched0();
    }
    __builtin_amdgcn_s_setprio(C_PRIO);
    {
      lptr q0 = pb0 + 4096, q1 = pb1 + 4096;
      F1.b[2] = trread2(q0 + 8192, q0 + 8192 + 512);
      F1.a[0] = *(LDSAS bf16x8*)(pa1);
      F1.a[1] = *(LDSAS bf16x8*)(pa1 + 2048);
      F1.a[2] = *(LDSAS bf16x8*)(pa1 + 4096);
      F1.a[3] = *(LDSAS bf16x8*)(pa1 + 6144);
      F1.b[3] = trread2(q1 + 8192, q1 + 8192 + 512);
      mm(F0, 2, 4);
#pragma unroll
      for (int i = 0; i < 8; ++i) {
        __builtin_amdgcn_sched_group_barrier(0x008, 1, 0);
        __builtin_amdgcn_sched_group_barrier(0x100, 1, 0);
      }
      sched0();
      F1.b[4] = trread2(q0 + 16384, q0 + 16384 + 512);
      F1.b[5] = trread2(q1 + 16384, q1 + 16384 + 512);
      F1.b[0] = trread2(q0, q0 + 512);
      F1.b[1] = trread2(q1, q1 + 512);
      mm(F1, 2, 4);
#pragma unroll
      for (int i = 0; i < 8; ++i) {
        __builtin_amdgcn_sched_group_barrier(0x008, 1, 0);
        __builtin_amdgcn_sched_group_barrier(0x100, 1, 0);
      }
      sched0();
      lset(smem, F_T15 + wid * 4, 1);
      sched0();
    }
    STAMP(2);
    u32x4 pkf[2][2], vf[2][2];
    f32x4 kvp[2][2], zz[2];
    const f32x4 zero4 = {0.f, 0.f, 0.f, 0.f};
    auto phik = [&](int u) {
#pragma unroll
      for (int c = 0; c < 2; ++c)
#pragma unroll
        for (int j = 0; j < 4; ++j) {
          const f32x4 kk = acc[2 + c][2 * u + (j >> 1)];
          pkf[u][c][j] = pack2(phi_f(kk[2 * (j & 1)]), phi_f(kk[2 * (j & 1) + 1]));
        }
    };
    auto packv = [&](int u) {
#pragma unroll
      for (int c = 0; c < 2; ++c)
#pragma unroll
        for (int j = 0; j < 4; ++j) {
          const f32x4 vv = acc[4 + c][2 * u + (j >> 1)];
          vf[u][c][j] = pack2(vv[2 * (j & 1)], vv[2 * (j & 1) + 1]);
        }
    };
    mm(F0, 4, 6);
    mm(F1, 4, 6);
    phik(0);
#pragma unroll
    for (int i = 0; i < 16; ++i) {
      __builtin_amdgcn_sched_group_barrier(0x008, 1, 0);
      __builtin_amdgcn_sched_group_barrier(0x402, 4, 0);
    }
    sched0();
    packv(0);
    sched0();
#pragma unroll
    for (int c = 0; c < 2; ++c) {
      kvp[c][1] = MFMA16(BC8(pkf[0][c]), BC8(vf[0][1]), zero4);
      kvp[c][0] = MFMA16(BC8(pkf[0][c]), BC8(vf[0][0]), zero4);
      zz[c] = MFMA16(BC8(pkf[0][c]), BC8(ones), zero4);
    }
    phik(1);
    packv(1);
#pragma unroll
    for (int i = 0; i < 6; ++i) {
      __builtin_amdgcn_sched_group_barrier(0x008, 1, 0);
      __builtin_amdgcn_sched_group_barrier(0x402, 5, 0);
    }
    sched0();
    kvp[0][1] = MFMA16(BC8(pkf[1][0]), BC8(vf[1][1]), kvp[0][1]);
    kvp[1][1] = MFMA16(BC8(pkf[1][1]), BC8(vf[1][1]), kvp[1][1]);
    zz[1] = MFMA16(BC8(pkf[1][1]), BC8(ones), zz[1]);
    sched0();
    kvp[0][0] = MFMA16(BC8(pkf[1][0]), BC8(vf[1][0]), kvp[0][0]);
    kvp[1][0] = MFMA16(BC8(pkf[1][1]), BC8(vf[1][0]), kvp[1][0]);
    zz[0] = MFMA16(BC8(pkf[1][0]), BC8(ones), zz[0]);
    sched0();
    {
      *(LDSAS u32x4*)(x1w) = u32x4{pack2(kvp[1][1][0], kvp[1][1][1]), pack2(kvp[1][1][2], kvp[1][1][3]), pack2(kvp[0][1][0], kvp[0][1][1]), pack2(kvp[0][1][2], kvp[0][1][3])};
      if (l15 < 2) *(LDSAS f32x4*)(x1zw) = l15 ? zz[0] : zz[1];
      lset(smem, F_X1 + wid * 4, 1);
      u32x4 r;
      f32x4 rz, rz0;
      spin<0>([&]() {
        const int f = *(volatile LDSAS int*)(smem + F_X1 + (wid ^ 2) * 4);
        r = *(volatile LDSAS u32x4*)(x1r);
        rz = *(volatile LDSAS f32x4*)(x1zr);
        rz0 = *(volatile LDSAS f32x4*)(x1zr + 64);
        return __builtin_amdgcn_readfirstlane(f) != 0;
      });
      sched0();
      st_sc1(u32x4{pack2(kvp[0][0][0] + bflo(r[0]), kvp[0][0][1] + bfhi(r[0])), pack2(kvp[0][0][2] + bflo(r[1]), kvp[0][0][3] + bfhi(r[1])),
                   pack2(kvp[1][0][0] + bflo(r[2]), kvp[1][0][1] + bfhi(r[2])), pack2(kvp[1][0][2] + bflo(r[3]), kvp[1][0][3] + bfhi(r[3]))}, rp, vkvp, 0);
      const f32x4 za = zz[0] + rz, zo = zz[1] + rz0;
      const u32x4 zs = {pack2(za[0], za[1]), pack2(za[2], za[3]), pack2(zo[0], zo[1]), pack2(zo[2], zo[3])};
      asm volatile("s_mov_b64 exec, %3\n\tbuffer_store_dwordx4 %0, %1, %2, 0 offen sc1\n\ts_mov_b64 exec, -1\n\ts_nop 0"
                   ::"v"(zs), "v"(vzp), "s"(rp), "s"(0x0003000300030003ull) : "memory");
      {
        unsigned old;
        asm volatile("s_mov_b64 exec, 1\n\tds_add_rtn_u32 %0, %1, %2\n\ts_mov_b64 exec, -1\n\ts_waitcnt lgkmcnt(0)"
                     : "=&v"(old) : "v"((unsigned)(size_t)smem + F_NP), "v"(1u) : "memory");
        if (__builtin_amdgcn_readfirstlane(old) == 3) post<16>(cntb + MB_P * 4, vpostP);
      }
      sched0();
    }
    STAMP(3);
    auto phiq = [&](int r0, int r1) {
#pragma unroll
      for (int rt = r0; rt < r1; ++rt) {
#pragma unroll
        for (int j = 0; j < 8; ++j) pq[rt][j] = phi_f(acc[j >> 2][rt][j & 3]);
        pqf[rt] = u32x4{pack2(pq[rt][0], pq[rt][1]), pack2(pq[rt][2], pq[rt][3]), pack2(pq[rt][4], pq[rt][5]), pack2(pq[rt][6], pq[rt][7])};
        asm volatile("" : "+v"(pqf[rt]));
        sched0();
      }
    };
    const int headq = hq * 2 + wh;
    const char* zb = P.ws + WS_KVP + (headq * 2 + wr) * 16 * KVP_SLOT + 1024;
    const char* pb = P.ws + WS_KVP + ((headq * 2 + wr) * 16 + 3) * KVP_SLOT;
    const char* pb2 = pb + 6 * KVP_SLOT;
    const char* pb3 = pb + 11 * KVP_SLOT;
    unsigned vz0 = g * 16 + l15 * KVP_SLOT, vo = lane * 16;
    static_assert((16 - 2 * W1N) % 3 == 0, "");
    if (W1N < 8 && wid) wo_dma(P, smem, nt, voW, 2 * W1N + (wid - 1) * ((16 - 2 * W1N) / 3), (16 - 2 * W1N) / 3);
    if (wid == 0) poll_prime<0>(mbx + MB_P * 4);
    sched0();
    mm(F0, 0, 2);
    mm(F1, 0, 2);
    sched0();
    phiq(0, PQ_PRE);
    if (wid == 0) poll_prime<1>(mbx + MB_P * 4);
    u32x4 sel0, sel1;
    {
      const unsigned one = g == (l15 >> 2) ? ((l15 & 1) ? 0x3f800000u : 0x00003f80u) : 0u;
      const unsigned e0 = (l15 & 2) ? 0u : one, e1 = (l15 & 2) ? one : 0u;
      sel0 = u32x4{e0, e1, 0u, 0u};
      sel1 = u32x4{0u, 0u, e0, e1};
    }
    asm volatile("" : "+s"(zb), "+s"(pb), "+s"(pb2), "+s"(pb3), "+v"(vz0), "+v"(vo), "+v"(sel0), "+v"(sel1));
    STAMP(4);
    group_wait<CSL, false>(smem, F_PR, wid == 0, mbx + MB_P * 4, 16);
    STAMP(5);
    auto pstage = [&](auto first_) {
      u32x4 z0, pr[16];
      LDG(z0, vz0, zb, 0);
      sfor<0, 16>([&](auto p_) {
        constexpr int p = decltype(p_)::v;
        ldg_sc1<(p - (p < 6 ? 3 : p < 12 ? 9 : 14)) * KVP_SLOT>(pr[p], vo, p < 6 ? pb : p < 12 ? pb2 : pb3);
      });
      sched0();
      if constexpr (decltype(first_)::v) phiq(PQ_PRE, 4);
      asm volatile("s_waitcnt vmcnt(16)" : "+v"(z0)::"memory");
      float z[8], rd[4];
#pragma unroll
      for (int j = 0; j < 4; ++j) { z[2 * j] = bflo(z0[j]); z[2 * j + 1] = bfhi(z0[j]); }
#pragma unroll
      for (int j = 0; j < 8; ++j) z[j] += row_ror<8>(z[j]);
#pragma unroll
      for (int j = 0; j < 8; ++j) z[j] += row_ror<4>(z[j]);
#pragma unroll
      for (int j = 0; j < 8; ++j) z[j] += row_ror<2>(z[j]);
#pragma unroll
      for (int j = 0; j < 8; ++j) z[j] += row_ror<1>(z[j]);
      const u32x4 zf = {pack2(z[0], z[1]), pack2(z[2], z[3]), pack2(z[4], z[5]), pack2(z[6], z[7])};
#pragma unroll
      for (int rt = 0; rt < 4; ++rt) {
        const f32x4 den = MFMA16(BC8(zf), BC8(pqf[rt]), zero4);
        rd[rt] = __builtin_amdgcn_rcpf(den[0] + 1e-6f);
      }
      s0 = zero4; s1 = zero4;
      sfor<0, 4>([&](auto i_) {
        constexpr int i = decltype(i_)::v;
        sched0();
        asm volatile("s_waitcnt vmcnt(%4)" : "+v"(pr[4 * i]), "+v"(pr[4 * i + 1]), "+v"(pr[4 * i + 2]), "+v"(pr[4 * i + 3]) : "i"(12 - 4 * i) : "memory");
        if constexpr (i == 3) { if (wid == 0) poll_prime<0>(mbx + MB_O * 4); }
#pragma unroll
        for (int p = 4 * i; p < 4 * i + 4; ++p) { s0 = MFMA16(BC8(sel0), BC8(pr[p]), s0); s1 = MFMA16(BC8(sel1), BC8(pr[p]), s1); }
        const float r = rd[i];
        pqs[i] = u32x4{pack2(pq[i][0] * r, pq[i][1] * r), pack2(pq[i][2] * r, pq[i][3] * r), pack2(pq[i][4] * r, pq[i][5] * r), pack2(pq[i][6] * r, pq[i][7] * r)};
        asm volatile("" : "+v"(pqs[i]));
#pragma unroll
        for (int j = 0; j < 8; ++j) {
          __builtin_amdgcn_sched_group_barrier(0x008, 1, 0);
          __builtin_amdgcn_sched_group_barrier(0x002, 2, 0);
        }
        sched0();
      });
      whv = *(volatile LDSAS u32x4*)(smem + F_HR);
      if (__builtin_expect(!any_lane(s0[0] != s0[0] || rd[0] != rd[0]), 1)) return true;
      bool m = has_mark(z0);
#pragma unroll
      for (int p = 0; p < 16; ++p) m |= has_mark(pr[p]);
      return !any_lane(m);
    };
    if (__builtin_expect(!pstage(IC<1>{}), 0)) {
#pragma unroll 1
      for (int att = 0; att < SPIN_LIMIT; ++att) {
        if (lane == 0) CNT_SLOW(0);
        if (pstage(IC<0>{})) break;
      }
    }
  }
  {
    const f32x4 zero4 = {0.f, 0.f, 0.f, 0.f};
    STAMP(6);
    *(LDSAS u32x4*)(xp) = u32x4{pack2(s0[0], s0[1]), pack2(s0[2], s0[3]), pack2(s1[0], s1[1]), pack2(s1[2], s1[3])};
    lset(smem, F_X2 + wid * 4, 1);
    if (__builtin_amdgcn_readfirstlane(whv[0] & whv[1] & whv[2] & whv[3]) == 0) spin<0>([&]() { return lmin4(smem, F_HR) != 0; });
    u32x2 k0a, k0b, k1a, k1b;
    spin<0>([&]() {
      const int f = *(volatile LDSAS int*)(smem + F_X2 + (wid ^ 2) * 4);
      k0a = *(volatile LDSAS u32x2*)(k0); k0b = *(volatile LDSAS u32x2*)(k0x); k1a = *(volatile LDSAS u32x2*)(k1); k1b = *(volatile LDSAS u32x2*)(k1x);
      return __builtin_amdgcn_readfirstlane(f) != 0;
    });
    sched0();
    const u32x4 kv0 = {k0a[0], k0a[1], k0b[0], k0b[1]}, kv1 = {k1a[0], k1a[1], k1b[0], k1b[1]};
    f32x4 n0[4], n1[4];
#pragma unroll
    for (int rt = 0; rt < 4; ++rt) {
      n0[rt] = MFMA16(BC8(kv0), BC8(pqs[rt]), zero4);
      n1[rt] = MFMA16(BC8(kv1), BC8(pqs[rt]), zero4);
    }
    sched0();
#pragma unroll
    for (int rt = 0; rt < 4; ++rt) {
      const u32x4 o = {pack2(n0[rt][0], n0[rt][1]), pack2(n0[rt][2], n0[rt][3]), pack2(n1[rt][0], n1[rt][1]), pack2(n1[rt][2], n1[rt][3])};
      st_sc1(o, ro, (rt >> 1) ? vst2 : vst, (rt & 1) * 1024);
      sched0();
    }
    if (wid == 0) { poll_prime<1>(mbx + MB_O * 4); waitv<1>(); } else waitv<0>();
    {
      unsigned old;
      asm volatile("s_mov_b64 exec, 1\n\tds_add_rtn_u32 %0, %1, %2\n\ts_mov_b64 exec, -1\n\ts_waitcnt lgkmcnt(0)"
                   : "=&v"(old) : "v"((unsigned)(size_t)smem + F_NO), "v"(1u) : "memory");
      if (wid == 0) {
        unsigned t;
        asm volatile("v_readfirstlane_b32 %0, v253" : "=s"(t)::"memory");
        oready = __builtin_amdgcn_readfirstlane(t) >= 12u;
        if (oready) lset(smem, F_OR, 1);
      }
      if (__builtin_amdgcn_readfirstlane(old) == 3) post<16>(cntb + ((hq & 6) == 2 ? MB_O2 : MB_O) * 4, vpostO);
    }
    STAMP(7);
  }
  {
    const int rb2 = G2MAP ? 2 * xl + (idx >> 4) : 4 * (xl >> 1) + (idx >> 3);
    const int wb = (4 * g + q4) * 128 + (p4 & 1) * 8 + (((p4 >> 1) ^ ((((g & 1) << 1) | (q4 >> 1)) << 1)) << 4);
    const char* ob = P.ws + WS_KVT + rb2 * 262144 + wid * 4096;
    unsigned vo = lane * 16;
    asm volatile("" : "+v"(vo));
    spin<0>([&]() { return lmin4(smem, F_X2) != 0; });
    if (wid) wo_dma(P, smem, nt, voW, 11 + wid * 5 + W3_EARLY, (wid == 3 ? 4 : 5) - W3_EARLY);
    f32x4 acc[4][2];
    {
      u32x4 of[OD];
      lptr w[4];
#pragma unroll
      for (int T = 0; T < 4; ++T) { w[T] = smem + (wb ^ (T << 5)); asm volatile("" : "+v"(w[T])); }
      bf16x8 a[3][4];
      u32x4 fl;
#pragma unroll
      for (int h = 0; h < 2; ++h)
#pragma unroll
        for (int T = 0; T < 4; ++T) { a[h][T] = trread2(w[T] + HD(h) * 4096, w[T] + HD(h) * 4096 + 2048); sched0(); }
#pragma unroll
      for (int i = 0; i < 4; ++i) { acc[i][0] = f32x4{0.f, 0.f, 0.f, 0.f}; acc[i][1] = acc[i][0]; asm volatile("" : "+v"(acc[i][0]), "+v"(acc[i][1])); }
      if (!oready) group_wait<CSL, false>(smem, F_OR, wid == 0, mbx + MB_O * 4, 12);
      __builtin_amdgcn_s_setprio(G2_PRIO);
      STAMP(8);
      auto ldo = [&](auto f_) {
        constexpr int f = decltype(f_)::v;
        ldg_sc1<(f & 3) * 1024>(of[f % OD], vo, ob + (AH(f >> 1) >> 1) * 16384);
      };
      auto check = [&](auto h_, auto n_) {
        constexpr int h = decltype(h_)::v;
        u32x4 &f0 = of[(2 * h) % OD], &f1 = of[(2 * h + 1) % OD];
        asm volatile("s_waitcnt vmcnt(%2)" : "+v"(f0), "+v"(f1) : "i"(decltype(n_)::v) : "memory");
      };
      sfor<0, OD>(ldo);
      check(IC<0>{}, IC<OD - 2>{});
      sfor<0, 32>([&](auto h_) {
        constexpr int h = decltype(h_)::v;
        constexpr int issued = 2 * h + OD < 64 ? 2 * h + OD : 64;
        __builtin_amdgcn_s_waitcnt(h == 31 ? 0xC07F : 0xC87F);
        sched0();
        if constexpr (h < 30 && (HD(h + 2) >> 4) != (HD(h + 1) >> 4)) {
#pragma unroll
          for (int T = 0; T < 4; ++T) { w[T] += HD(h + 2) >= 16 ? 65536 : -65536; asm volatile("" : "+v"(w[T])); }
        }
        if constexpr (h == 0) { lset(smem, F_WC + wid * 4, 1); sched0(); }
        if constexpr (h == 14 || h == 28) {
          if (__builtin_expect(__builtin_amdgcn_readfirstlane(fl[0] & fl[1] & fl[2] & fl[3]) == 0, 0))
            spin<1>([&]() { return lmin4(smem, h == 14 ? F_WC : F_WL) != 0; });
          sched0();
        }
        if constexpr (h == 13 || h == 27) { fl = *(volatile LDSAS u32x4*)(smem + (h == 13 ? F_WC : F_WL)); sched0(); }
        constexpr int o = (HD(h < 30 ? h + 2 : 0) & 15) * 4096;
        auto half = [&](int T0) {
#pragma unroll
          for (int T = T0; T < T0 + 2; ++T) {
            if constexpr (h < 30) a[(h + 2) % 3][T] = trread2(w[T] + o, w[T] + o + 2048);
            acc[T][0] = MFMA16(a[h % 3][T], BC8(of[(2 * h) % OD]), acc[T][0]);
            acc[T][1] = MFMA16(a[h % 3][T], BC8(of[(2 * h + 1) % OD]), acc[T][1]);
          }
#pragma unroll
          for (int j = 0; j < 4; ++j) {
            __builtin_amdgcn_sched_group_barrier(0x008, 1, 0);
            __builtin_amdgcn_sched_group_barrier(0x100, 1, 0);
          }
          sched0();
        };
        half(0);
        constexpr int LS = 24 - OD / 2;
        static_assert(O2_AT < LS && LS <= O2_AT + OD / 2 - 1, "");
        if constexpr (h < 31) check(IC<h + 1>{}, IC<issued - 2 * h - 4 + (h > O2_AT && h <= LS)>{});
        sched0();
        half(2);
        if constexpr (h == LS) {
          unsigned t;
          asm volatile("s_waitcnt vmcnt(%1)\n\tv_readfirstlane_b32 %0, v253" : "=s"(t) : "i"(2 * (LS - 1 - O2_AT)) : "memory");
          if (__builtin_expect(__builtin_amdgcn_readfirstlane(t) < 4u, 0)) { if (lane == 0) CNT_SLOW(3); group_wait<0, true>(smem, F_OR, true, mbx + MB_O2 * 4, 4); }
          sched0();
        }
        if constexpr (2 * h + OD < 64) { ldo(IC<2 * h + OD>{}); ldo(IC<2 * h + OD + 1>{}); }
        if constexpr (h == O2_AT) poll_prime<0>(mbx + MB_O2 * 4);
        sched0();
      });
    }
    STAMP(9);
#pragma unroll
    for (int st = 0; st < 2; ++st) {
      const unsigned dst = ((rb2 * 128 + wid * 32 + st * 16 + l15) * 2048 + 1024 + nt * 64 + 16 * (g & 1) + 8 * (g >> 1)) * 2;
#pragma unroll
      for (int tp = 0; tp < 2; ++tp) {
        const f32x4 u = acc[2 * tp][st], v = acc[2 * tp + 1][st];
        auto r0 = __builtin_amdgcn_permlane16_swap(pack2(u[0], u[1]), pack2(v[0], v[1]), false, false);
        auto r1 = __builtin_amdgcn_permlane16_swap(pack2(u[2], u[3]), pack2(v[2], v[3]), false, false);
        const u32x4 o = {r0[0], r1[0], r0[1], r1[1]};
        st_out<OUTAUX>(o, P.out, dst + 64 * tp);
      }
    }
  }
  STAMP(10);
  ST_OUT;
  PAD_END;
}

#define CONV_ASM \
  "s_mov_b32 vcc_lo, 0\n\ts_mov_b32 vcc_hi, 0x80000000\n\ts_mov_b64 s[40:41], %14\n\t" \
  "s_mov_b32 s42, %15\n\tds_read_b64_tr_b16 v[34:35], %8 offset:0\n\tds_read_b64_tr_b16 v[36:37], %8 offset:32\n\t" \
  "ds_read_b64_tr_b16 v[98:99], %4 offset:0\n\tds_read_b64_tr_b16 v[100:101], %5 offset:0\n\tds_read_b64_tr_b16 v[102:103], %6 offset:0\n\t" \
  "ds_read_b64_tr_b16 v[104:105], %7 offset:0\n\tds_read_b64_tr_b16 v[38:39], %8 offset:256\n\tds_read_b64_tr_b16 v[40:41], %8 offset:288\n\t" \
  "ds_read_b64_tr_b16 v[106:107], %4 offset:1024\n\tds_read_b64_tr_b16 v[108:109], %5 offset:1024\n\tds_read_b64_tr_b16 v[110:111], %6 offset:1024\n\t" \
  "ds_read_b64_tr_b16 v[112:113], %7 offset:1024\n\tds_read_b64_tr_b16 v[42:43], %8 offset:512\n\tds_read_b64_tr_b16 v[44:45], %8 offset:544\n\t" \
  "ds_read_b64_tr_b16 v[114:115], %4 offset:2048\n\tds_read_b64_tr_b16 v[116:117], %5 offset:2048\n\tds_read_b64_tr_b16 v[118:119], %6 offset:2048\n\t" \
  "ds_read_b64_tr_b16 v[120:121], %7 offset:2048\n\tv_mov_b32 v247, 0\n\tv_mov_b32 v248, 0\n\t" \
  "v_mov_b32 v249, 0\n\ts_waitcnt lgkmcnt(14)\n\tv_mfma_f32_16x16x32_bf16 v[226:229], v[98:101], v[34:37], 0\n\t" \
  "ds_read_b64_tr_b16 v[46:47], %8 offset:768\n\tds_read_b64_tr_b16 v[48:49], %8 offset:800\n\tds_read_b64_tr_b16 v[122:123], %4 offset:3072\n\t" \
  "ds_read_b64_tr_b16 v[124:125], %5 offset:3072\n\tds_read_b64_tr_b16 v[126:127], %6 offset:3072\n\tds_read_b64_tr_b16 v[128:129], %7 offset:3072\n\t" \
  "s_waitcnt lgkmcnt(15)\n\tv_mfma_f32_16x16x32_bf16 v[230:233], v[102:105], v[34:37], 0\n\ts_waitcnt lgkmcnt(14)\n\t" \
  "v_mfma_f32_16x16x32_bf16 v[234:237], v[106:109], v[34:37], 0\n\tds_read_b64_tr_b16 v[50:51], %8 offset:1024\n\tds_read_b64_tr_b16 v[52:53], %8 offset:1056\n\t" \
  "ds_read_b64_tr_b16 v[130:131], %4 offset:4096\n\tds_read_b64_tr_b16 v[132:133], %5 offset:4096\n\tds_read_b64_tr_b16 v[134:135], %6 offset:4096\n\t" \
  "ds_read_b64_tr_b16 v[136:137], %7 offset:4096\n\ts_waitcnt lgkmcnt(15)\n\tv_mfma_f32_16x16x32_bf16 v[238:241], v[110:113], v[34:37], 0\n\t" \
  "v_cndmask_b32_e32 v250, v229, v249, vcc\n\tv_cndmask_b32_e32 v251, v233, v229, vcc\n\ts_nop 0\n\t" \
  "v_add_f32_dpp v228, v250, v228 wave_ror:1 row_mask:0xf bank_mask:0xf bound_ctrl:1\n\tv_add_f32_dpp v232, v251, v232 wave_ror:1 row_mask:0xf bank_mask:0xf bound_ctrl:1\n\tv_mfma_f32_16x16x32_bf16 v[234:237], v[98:101], v[38:41], v[234:237]\n\t" \
  "v_cndmask_b32_e32 v250, v228, v248, vcc\n\tv_cndmask_b32_e32 v251, v232, v228, vcc\n\ts_nop 0\n\t" \
  "v_add_f32_dpp v227, v250, v227 wave_ror:1 row_mask:0xf bank_mask:0xf bound_ctrl:1\n\tv_add_f32_dpp v231, v251, v231 wave_ror:1 row_mask:0xf bank_mask:0xf bound_ctrl:1\n\tv_cndmask_b32_e32 v250, v227, v247, vcc\n\t" \
  "v_cndmask_b32_e32 v251, v231, v227, vcc\n\tv_mfma_f32_16x16x32_bf16 v[238:241], v[102:105], v[38:41], v[238:241]\n\tv_add_f32_dpp v226, v250, v226 wave_ror:1 row_mask:0xf bank_mask:0xf bound_ctrl:1\n\t" \
  "v_add_f32_dpp v230, v251, v230 wave_ror:1 row_mask:0xf bank_mask:0xf bound_ctrl:1\n\tv_cvt_pk_bf16_f32 v252, v226, v230\n\tv_mfma_f32_16x16x32_bf16 v[242:245], v[106:109], v[38:41], 0\n\t" \
  "ds_read_b64_tr_b16 v[54:55], %8 offset:1280\n\tds_read_b64_tr_b16 v[56:57], %8 offset:1312\n\tds_read_b64_tr_b16 v[138:139], %4 offset:5120\n\t" \
  "ds_read_b64_tr_b16 v[140:141], %5 offset:5120\n\tv_mfma_f32_16x16x32_bf16 v[246:249], v[110:113], v[38:41], 0\n\tds_read_b64_tr_b16 v[142:143], %6 offset:5120\n\t" \
  "ds_read_b64_tr_b16 v[144:145], %7 offset:5120\n\tv_cndmask_b32_e32 v250, v237, v233, vcc\n\tv_cndmask_b32_e32 v251, v241, v237, vcc\n\t" \
  "s_waitcnt lgkmcnt(15)\n\tv_mfma_f32_16x16x32_bf16 v[242:245], v[114:117], v[34:37], v[242:245]\n\tv_add_f32_dpp v236, v250, v236 wave_ror:1 row_mask:0xf bank_mask:0xf bound_ctrl:1\n\t" \
  "v_add_f32_dpp v240, v251, v240 wave_ror:1 row_mask:0xf bank_mask:0xf bound_ctrl:1\n\tv_mfma_f32_16x16x32_bf16 v[246:249], v[118:121], v[34:37], v[246:249]\n\tv_cndmask_b32_e32 v250, v236, v232, vcc\n\t" \
  "v_cndmask_b32_e32 v251, v240, v236, vcc\n\ts_nop 0\n\tv_add_f32_dpp v235, v250, v235 wave_ror:1 row_mask:0xf bank_mask:0xf bound_ctrl:1\n\t" \
  "v_add_f32_dpp v239, v251, v239 wave_ror:1 row_mask:0xf bank_mask:0xf bound_ctrl:1\n\tv_mfma_f32_16x16x32_bf16 v[242:245], v[98:101], v[42:45], v[242:245]\n\tv_cndmask_b32_e32 v250, v235, v231, vcc\n\t" \
  "v_cndmask_b32_e32 v251, v239, v235, vcc\n\ts_nop 0\n\tv_add_f32_dpp v234, v250, v234 wave_ror:1 row_mask:0xf bank_mask:0xf bound_ctrl:1\n\t" \
  "v_add_f32_dpp v238, v251, v238 wave_ror:1 row_mask:0xf bank_mask:0xf bound_ctrl:1\n\tv_mfma_f32_16x16x32_bf16 v[246:249], v[102:105], v[42:45], v[246:249]\n\tv_cvt_pk_bf16_f32 v253, v234, v238\n\t" \
  "v_mfma_f32_16x16x32_bf16 v[226:229], v[114:117], v[38:41], 0\n\tds_read_b64_tr_b16 v[58:59], %8 offset:1536\n\tds_read_b64_tr_b16 v[60:61], %8 offset:1568\n\t" \
  "ds_read_b64_tr_b16 v[146:147], %4 offset:6144\n\tds_read_b64_tr_b16 v[148:149], %5 offset:6144\n\tv_mfma_f32_16x16x32_bf16 v[230:233], v[118:121], v[38:41], 0\n\t" \
  "ds_read_b64_tr_b16 v[150:151], %6 offset:6144\n\tds_read_b64_tr_b16 v[152:153], %7 offset:6144\n\tv_mfma_f32_16x16x32_bf16 v[226:229], v[106:109], v[42:45], v[226:229]\n\t" \
  "v_cndmask_b32_e32 v250, v245, v241, vcc\n\tv_cndmask_b32_e32 v251, v249, v245, vcc\n\tv_mfma_f32_16x16x32_bf16 v[230:233], v[110:113], v[42:45], v[230:233]\n\t" \
  "v_add_f32_dpp v244, v250, v244 wave_ror:1 row_mask:0xf bank_mask:0xf bound_ctrl:1\n\tv_add_f32_dpp v248, v251, v248 wave_ror:1 row_mask:0xf bank_mask:0xf bound_ctrl:1\n\ts_waitcnt lgkmcnt(15)\n\t" \
  "v_mfma_f32_16x16x32_bf16 v[226:229], v[122:125], v[34:37], v[226:229]\n\tv_cndmask_b32_e32 v250, v244, v240, vcc\n\tv_cndmask_b32_e32 v251, v248, v244, vcc\n\t" \
  "s_nop 0\n\tv_add_f32_dpp v243, v250, v243 wave_ror:1 row_mask:0xf bank_mask:0xf bound_ctrl:1\n\tv_add_f32_dpp v247, v251, v247 wave_ror:1 row_mask:0xf bank_mask:0xf bound_ctrl:1\n\t" \
  "v_mfma_f32_16x16x32_bf16 v[230:233], v[126:129], v[34:37], v[230:233]\n\tv_cndmask_b32_e32 v250, v243, v239, vcc\n\tv_cndmask_b32_e32 v251, v247, v243, vcc\n\t" \
  "v_mfma_f32_16x16x32_bf16 v[226:229], v[98:101], v[46:49], v[226:229]\n\tv_add_f32_dpp v242, v250, v242 wave_ror:1 row_mask:0xf bank_mask:0xf bound_ctrl:1\n\tv_add_f32_dpp v246, v251, v246 wave_ror:1 row_mask:0xf bank_mask:0xf bound_ctrl:1\n\t" \
  "v_mfma_f32_16x16x32_bf16 v[230:233], v[102:105], v[46:49], v[230:233]\n\tv_cvt_pk_bf16_f32 v254, v242, v246\n\tv_mfma_f32_16x16x32_bf16 v[234:237], v[122:125], v[38:41], 0\n\t" \
  "ds_read_b64_tr_b16 v[62:63], %8 offset:1792\n\tds_read_b64_tr_b16 v[64:65], %8 offset:1824\n\tv_mfma_f32_16x16x32_bf16 v[238:241], v[126:129], v[38:41], 0\n\t" \
  "ds_read_b64_tr_b16 v[154:155], %4 offset:7168\n\tds_read_b64_tr_b16 v[156:157], %5 offset:7168\n\tv_mfma_f32_16x16x32_bf16 v[234:237], v[114:117], v[42:45], v[234:237]\n\t" \
  "ds_read_b64_tr_b16 v[158:159], %6 offset:7168\n\tds_read_b64_tr_b16 v[160:161], %7 offset:7168\n\tv_mfma_f32_16x16x32_bf16 v[238:241], v[118:121], v[42:45], v[238:241]\n\t" \
  "v_cndmask_b32_e32 v250, v229, v249, vcc\n\tv_cndmask_b32_e32 v251, v233, v229, vcc\n\tv_mfma_f32_16x16x32_bf16 v[234:237], v[106:109], v[46:49], v[234:237]\n\t" \
  "v_add_f32_dpp v228, v250, v228 wave_ror:1 row_mask:0xf bank_mask:0xf bound_ctrl:1\n\tv_add_f32_dpp v232, v251, v232 wave_ror:1 row_mask:0xf bank_mask:0xf bound_ctrl:1\n\tv_mfma_f32_16x16x32_bf16 v[238:241], v[110:113], v[46:49], v[238:241]\n\t" \
  "v_cndmask_b32_e32 v250, v228, v248, vcc\n\tv_cndmask_b32_e32 v251, v232, v228, vcc\n\ts_waitcnt lgkmcnt(15)\n\t" \
  "v_mfma_f32_16x16x32_bf16 v[234:237], v[130:133], v[34:37], v[234:237]\n\tv_add_f32_dpp v227, v250, v227 wave_ror:1 row_mask:0xf bank_mask:0xf bound_ctrl:1\n\tv_add_f32_dpp v231, v251, v231 wave_ror:1 row_mask:0xf bank_mask:0xf bound_ctrl:1\n\t" \
  "v_mfma_f32_16x16x32_bf16 v[238:241], v[134:137], v[34:37], v[238:241]\n\tv_cndmask_b32_e32 v250, v227, v247, vcc\n\tv_cndmask_b32_e32 v251, v231, v227, vcc\n\t" \
  "v_mfma_f32_16x16x32_bf16 v[234:237], v[98:101], v[50:53], v[234:237]\n\tv_add_f32_dpp v226, v250, v226 wave_ror:1 row_mask:0xf bank_mask:0xf bound_ctrl:1\n\tv_add_f32_dpp v230, v251, v230 wave_ror:1 row_mask:0xf bank_mask:0xf bound_ctrl:1\n\t" \
  "v_mfma_f32_16x16x32_bf16 v[238:241], v[102:105], v[50:53], v[238:241]\n\tv_cvt_pk_bf16_f32 v255, v226, v230\n\tbuffer_store_dwordx4 v[252:255], %9, %13, 0 offen offset:0 sc1\n\t" \
  "v_mfma_f32_16x16x32_bf16 v[242:245], v[130:133], v[38:41], 0\n\tds_read_b64_tr_b16 v[66:67], %8 offset:2048\n\tds_read_b64_tr_b16 v[68:69], %8 offset:2080\n\t" \
  "v_mfma_f32_16x16x32_bf16 v[246:249], v[134:137], v[38:41], 0\n\tds_read_b64_tr_b16 v[162:163], %4 offset:8192\n\tds_read_b64_tr_b16 v[164:165], %5 offset:8192\n\t" \
  "v_mfma_f32_16x16x32_bf16 v[242:245], v[122:125], v[42:45], v[242:245]\n\tds_read_b64_tr_b16 v[166:167], %6 offset:8192\n\tds_read_b64_tr_b16 v[168:169], %7 offset:8192\n\t" \
  "v_mfma_f32_16x16x32_bf16 v[246:249], v[126:129], v[42:45], v[246:249]\n\tv_cndmask_b32_e32 v250, v237, v233, vcc\n\tv_cndmask_b32_e32 v251, v241, v237, vcc\n\t" \
  "v_mfma_f32_16x16x32_bf16 v[242:245], v[114:117], v[46:49], v[242:245]\n\tv_add_f32_dpp v236, v250, v236 wave_ror:1 row_mask:0xf bank_mask:0xf bound_ctrl:1\n\tv_add_f32_dpp v240, v251, v240 wave_ror:1 row_mask:0xf bank_mask:0xf bound_ctrl:1\n\t" \
  "v_mfma_f32_16x16x32_bf16 v[246:249], v[118:121], v[46:49], v[246:249]\n\tv_cndmask_b32_e32 v250, v236, v232, vcc\n\tv_cndmask_b32_e32 v251, v240, v236, vcc\n\t" \
  "v_mfma_f32_16x16x32_bf16 v[242:245], v[106:109], v[50:53], v[242:245]\n\tv_add_f32_dpp v235, v250, v235 wave_ror:1 row_mask:0xf bank_mask:0xf bound_ctrl:1\n\tv_add_f32_dpp v239, v251, v239 wave_ror:1 row_mask:0xf bank_mask:0xf bound_ctrl:1\n\t" \
  "v_mfma_f32_16x16x32_bf16 v[246:249], v[110:113], v[50:53], v[246:249]\n\ts_waitcnt lgkmcnt(15)\n\tv_mfma_f32_16x16x32_bf16 v[242:245], v[138:141], v[34:37], v[242:245]\n\t" \
  "v_cndmask_b32_e32 v250, v235, v231, vcc\n\tv_cndmask_b32_e32 v251, v239, v235, vcc\n\tv_mfma_f32_16x16x32_bf16 v[246:249], v[142:145], v[34:37], v[246:249]\n\t" \
  "v_add_f32_dpp v234, v250, v234 wave_ror:1 row_mask:0xf bank_mask:0xf bound_ctrl:1\n\tv_add_f32_dpp v238, v251, v238 wave_ror:1 row_mask:0xf bank_mask:0xf bound_ctrl:1\n\tv_mfma_f32_16x16x32_bf16 v[242:245], v[98:101], v[54:57], v[242:245]\n\t" \
  "v_cvt_pk_bf16_f32 v252, v234, v238\n\tv_mfma_f32_16x16x32_bf16 v[246:249], v[102:105], v[54:57], v[246:249]\n\tv_mfma_f32_16x16x32_bf16 v[226:229], v[138:141], v[38:41], 0\n\t" \
  "ds_read_b64_tr_b16 v[70:71], %8 offset:2304\n\tds_read_b64_tr_b16 v[72:73], %8 offset:2336\n\tv_mfma_f32_16x16x32_bf16 v[230:233], v[142:145], v[38:41], 0\n\t" \
  "ds_read_b64_tr_b16 v[170:171], %4 offset:9216\n\tds_read_b64_tr_b16 v[172:173], %5 offset:9216\n\tv_mfma_f32_16x16x32_bf16 v[226:229], v[130:133], v[42:45], v[226:229]\n\t" \
  "ds_read_b64_tr_b16 v[174:175], %6 offset:9216\n\tds_read_b64_tr_b16 v[176:177], %7 offset:9216\n\tv_mfma_f32_16x16x32_bf16 v[230:233], v[134:137], v[42:45], v[230:233]\n\t" \
  "v_cndmask_b32_e32 v250, v245, v241, vcc\n\tv_cndmask_b32_e32 v251, v249, v245, vcc\n\tv_mfma_f32_16x16x32_bf16 v[226:229], v[122:125], v[46:49], v[226:229]\n\t" \
  "v_add_f32_dpp v244, v250, v244 wave_ror:1 row_mask:0xf bank_mask:0xf bound_ctrl:1\n\tv_add_f32_dpp v248, v251, v248 wave_ror:1 row_mask:0xf bank_mask:0xf bound_ctrl:1\n\tv_mfma_f32_16x16x32_bf16 v[230:233], v[126:129], v[46:49], v[230:233]\n\t" \
  "v_mfma_f32_16x16x32_bf16 v[226:229], v[114:117], v[50:53], v[226:229]\n\tv_cndmask_b32_e32 v250, v244, v240, vcc\n\tv_cndmask_b32_e32 v251, v248, v244, vcc\n\t" \
  "v_mfma_f32_16x16x32_bf16 v[230:233], v[118:121], v[50:53], v[230:233]\n\tv_add_f32_dpp v243, v250, v243 wave_ror:1 row_mask:0xf bank_mask:0xf bound_ctrl:1\n\tv_add_f32_dpp v247, v251, v247 wave_ror:1 row_mask:0xf bank_mask:0xf bound_ctrl:1\n\t" \
  "v_mfma_f32_16x16x32_bf16 v[226:229], v[106:109], v[54:57], v[226:229]\n\tv_mfma_f32_16x16x32_bf16 v[230:233], v[110:113], v[54:57], v[230:233]\n\tv_cndmask_b32_e32 v250, v243, v239, vcc\n\t" \
  "v_cndmask_b32_e32 v251, v247, v243, vcc\n\ts_waitcnt lgkmcnt(15)\n\tv_mfma_f32_16x16x32_bf16 v[226:229], v[146:149], v[34:37], v[226:229]\n\t" \
  "v_add_f32_dpp v242, v250, v242 wave_ror:1 row_mask:0xf bank_mask:0xf bound_ctrl:1\n\tv_add_f32_dpp v246, v251, v246 wave_ror:1 row_mask:0xf bank_mask:0xf bound_ctrl:1\n\tv_mfma_f32_16x16x32_bf16 v[230:233], v[150:153], v[34:37], v[230:233]\n\t" \
  "v_mfma_f32_16x16x32_bf16 v[226:229], v[98:101], v[58:61], v[226:229]\n\tv_cvt_pk_bf16_f32 v253, v242, v246\n\tv_mfma_f32_16x16x32_bf16 v[230:233], v[102:105], v[58:61], v[230:233]\n\t" \
  "v_mfma_f32_16x16x32_bf16 v[234:237], v[146:149], v[38:41], 0\n\tds_read_b64_tr_b16 v[74:75], %8 offset:2560\n\tds_read_b64_tr_b16 v[76:77], %8 offset:2592\n\t" \
  "v_mfma_f32_16x16x32_bf16 v[238:241], v[150:153], v[38:41], 0\n\tds_read_b64_tr_b16 v[178:179], %4 offset:10240\n\tds_read_b64_tr_b16 v[180:181], %5 offset:10240\n\t" \
  "v_mfma_f32_16x16x32_bf16 v[234:237], v[138:141], v[42:45], v[234:237]\n\tds_read_b64_tr_b16 v[182:183], %6 offset:10240\n\tds_read_b64_tr_b16 v[184:185], %7 offset:10240\n\t" \
  "v_mfma_f32_16x16x32_bf16 v[238:241], v[142:145], v[42:45], v[238:241]\n\tv_cndmask_b32_e32 v250, v229, v249, vcc\n\tv_cndmask_b32_e32 v251, v233, v229, vcc\n\t" \
  "v_mfma_f32_16x16x32_bf16 v[234:237], v[130:133], v[46:49], v[234:237]\n\tv_add_f32_dpp v228, v250, v228 wave_ror:1 row_mask:0xf bank_mask:0xf bound_ctrl:1\n\tv_add_f32_dpp v232, v251, v232 wave_ror:1 row_mask:0xf bank_mask:0xf bound_ctrl:1\n\t" \
  "v_mfma_f32_16x16x32_bf16 v[238:241], v[134:137], v[46:49], v[238:241]\n\tv_mfma_f32_16x16x32_bf16 v[234:237], v[122:125], v[50:53], v[234:237]\n\tv_cndmask_b32_e32 v250, v228, v248, vcc\n\t" \
  "v_cndmask_b32_e32 v251, v232, v228, vcc\n\tv_mfma_f32_16x16x32_bf16 v[238:241], v[126:129], v[50:53], v[238:241]\n\tv_mfma_f32_16x16x32_bf16 v[234:237], v[114:117], v[54:57], v[234:237]\n\t" \
  "v_add_f32_dpp v227, v250, v227 wave_ror:1 row_mask:0xf bank_mask:0xf bound_ctrl:1\n\tv_add_f32_dpp v231, v251, v231 wave_ror:1 row_mask:0xf bank_mask:0xf bound_ctrl:1\n\tv_mfma_f32_16x16x32_bf16 v[238:241], v[118:121], v[54:57], v[238:241]\n\t" \
  "v_mfma_f32_16x16x32_bf16 v[234:237], v[106:109], v[58:61], v[234:237]\n\tv_cndmask_b32_e32 v250, v227, v247, vcc\n\tv_cndmask_b32_e32 v251, v231, v227, vcc\n\t" \
  "v_mfma_f32_16x16x32_bf16 v[238:241], v[110:113], v[58:61], v[238:241]\n\ts_waitcnt lgkmcnt(15)\n\tv_mfma_f32_16x16x32_bf16 v[234:237], v[154:157], v[34:37], v[234:237]\n\t" \
  "v_add_f32_dpp v226, v250, v226 wave_ror:1 row_mask:0xf bank_mask:0xf bound_ctrl:1\n\tv_add_f32_dpp v230, v251, v230 wave_ror:1 row_mask:0xf bank_mask:0xf bound_ctrl:1\n\tv_mfma_f32_16x16x32_bf16 v[238:241], v[158:161], v[34:37], v[238:241]\n\t" \
  "v_mfma_f32_16x16x32_bf16 v[234:237], v[98:101], v[62:65], v[234:237]\n\tv_cvt_pk_bf16_f32 v254, v226, v230\n\tv_mfma_f32_16x16x32_bf16 v[238:241], v[102:105], v[62:65], v[238:241]\n\t" \
  "v_mfma_f32_16x16x32_bf16 v[242:245], v[154:157], v[38:41], 0\n\tds_read_b64_tr_b16 v[78:79], %8 offset:2816\n\tds_read_b64_tr_b16 v[80:81], %8 offset:2848\n\t" \
  "v_mfma_f32_16x16x32_bf16 v[246:249], v[158:161], v[38:41], 0\n\tds_read_b64_tr_b16 v[186:187], %4 offset:11264\n\tds_read_b64_tr_b16 v[188:189], %5 offset:11264\n\t" \
  "v_mfma_f32_16x16x32_bf16 v[242:245], v[146:149], v[42:45], v[242:245]\n\tds_read_b64_tr_b16 v[190:191], %6 offset:11264\n\tds_read_b64_tr_b16 v[192:193], %7 offset:11264\n\t" \
  "v_mfma_f32_16x16x32_bf16 v[246:249], v[150:153], v[42:45], v[246:249]\n\tv_cndmask_b32_e32 v250, v237, v233, vcc\n\tv_cndmask_b32_e32 v251, v241, v237, vcc\n\t" \
  "v_mfma_f32_16x16x32_bf16 v[242:245], v[138:141], v[46:49], v[242:245]\n\tv_add_f32_dpp v236, v250, v236 wave_ror:1 row_mask:0xf bank_mask:0xf bound_ctrl:1\n\tv_add_f32_dpp v240, v251, v240 wave_ror:1 row_mask:0xf bank_mask:0xf bound_ctrl:1\n\t" \
  "v_mfma_f32_16x16x32_bf16 v[246:249], v[142:145], v[46:49], v[246:249]\n\tv_cndmask_b32_e32 v250, v236, v232, vcc\n\tv_cndmask_b32_e32 v251, v240, v236, vcc\n\t" \
  "v_mfma_f32_16x16x32_bf16 v[242:245], v[130:133], v[50:53], v[242:245]\n\tv_mfma_f32_16x16x32_bf16 v[246:249], v[134:137], v[50:53], v[246:249]\n\tv_add_f32_dpp v235, v250, v235 wave_ror:1 row_mask:0xf bank_mask:0xf bound_ctrl:1\n\t" \
  "v_add_f32_dpp v239, v251, v239 wave_ror:1 row_mask:0xf bank_mask:0xf bound_ctrl:1\n\tv_mfma_f32_16x16x32_bf16 v[242:245], v[122:125], v[54:57], v[242:245]\n\tv_cndmask_b32_e32 v250, v235, v231, vcc\n\t" \
  "v_cndmask_b32_e32 v251, v239, v235, vcc\n\tv_mfma_f32_16x16x32_bf16 v[246:249], v[126:129], v[54:57], v[246:249]\n\tv_add_f32_dpp v234, v250, v234 wave_ror:1 row_mask:0xf bank_mask:0xf bound_ctrl:1\n\t" \
  "v_add_f32_dpp v238, v251, v238 wave_ror:1 row_mask:0xf bank_mask:0xf bound_ctrl:1\n\tv_mfma_f32_16x16x32_bf16 v[242:245], v[114:117], v[58:61], v[242:245]\n\tv_mfma_f32_16x16x32_bf16 v[246:249], v[118:121], v[58:61], v[246:249]\n\t" \
  "v_cvt_pk_bf16_f32 v255, v234, v238\n\tbuffer_store_dwordx4 v[252:255], %9, %13, 0 offen offset:1024 sc1\n\tv_mfma_f32_16x16x32_bf16 v[242:245], v[106:109], v[62:65], v[242:245]\n\t" \
  "s_mov_b64 s[40:41], %16\n\tglobal_load_dwordx4 %0, %11, s[40:41]\n\tv_mfma_f32_16x16x32_bf16 v[246:249], v[110:113], v[62:65], v[246:249]\n\t" \
  "s_add_u32 s40, s40, 0x20000\n\ts_addc_u32 s41, s41, 0\n\tglobal_load_dwordx4 %1, %11, s[40:41]\n\t" \
  "s_waitcnt lgkmcnt(15)\n\tv_mfma_f32_16x16x32_bf16 v[242:245], v[162:165], v[34:37], v[242:245]\n\tv_mfma_f32_16x16x32_bf16 v[246:249], v[166:169], v[34:37], v[246:249]\n\t" \
  "s_add_u32 s40, s40, 0x20000\n\ts_addc_u32 s41, s41, 0\n\tglobal_load_dwordx4 %2, %11, s[40:41]\n\t" \
  "v_mfma_f32_16x16x32_bf16 v[242:245], v[98:101], v[66:69], v[242:245]\n\ts_add_u32 s40, s40, 0x20000\n\ts_addc_u32 s41, s41, 0\n\t" \
  "global_load_dwordx4 %3, %11, s[40:41]\n\tv_mfma_f32_16x16x32_bf16 v[246:249], v[102:105], v[66:69], v[246:249]\n\tv_mfma_f32_16x16x32_bf16 v[226:229], v[162:165], v[38:41], 0\n\t" \
  "ds_read_b64_tr_b16 v[82:83], %8 offset:3072\n\tds_read_b64_tr_b16 v[84:85], %8 offset:3104\n\tv_mfma_f32_16x16x32_bf16 v[230:233], v[166:169], v[38:41], 0\n\t" \
  "ds_read_b64_tr_b16 v[194:195], %4 offset:12288\n\tds_read_b64_tr_b16 v[196:197], %5 offset:12288\n\tv_mfma_f32_16x16x32_bf16 v[226:229], v[154:157], v[42:45], v[226:229]\n\t" \
  "ds_read_b64_tr_b16 v[198:199], %6 offset:12288\n\tds_read_b64_tr_b16 v[200:201], %7 offset:12288\n\tv_mfma_f32_16x16x32_bf16 v[230:233], v[158:161], v[42:45], v[230:233]\n\t" \
  "v_cndmask_b32_e32 v250, v245, v241, vcc\n\tv_cndmask_b32_e32 v251, v249, v245, vcc\n\tv_mfma_f32_16x16x32_bf16 v[226:229], v[146:149], v[46:49], v[226:229]\n\t" \
  "v_mfma_f32_16x16x32_bf16 v[230:233], v[150:153], v[46:49], v[230:233]\n\tv_add_f32_dpp v244, v250, v244 wave_ror:1 row_mask:0xf bank_mask:0xf bound_ctrl:1\n\tv_add_f32_dpp v248, v251, v248 wave_ror:1 row_mask:0xf bank_mask:0xf bound_ctrl:1\n\t" \
  "v_mfma_f32_16x16x32_bf16 v[226:229], v[138:141], v[50:53], v[226:229]\n\tv_mfma_f32_16x16x32_bf16 v[230:233], v[142:145], v[50:53], v[230:233]\n\tv_cndmask_b32_e32 v250, v244, v240, vcc\n\t" \
  "v_cndmask_b32_e32 v251, v248, v244, vcc\n\tv_mfma_f32_16x16x32_bf16 v[226:229], v[130:133], v[54:57], v[226:229]\n\tv_mfma_f32_16x16x32_bf16 v[230:233], v[134:137], v[54:57], v[230:233]\n\t" \
  "v_mfma_f32_16x16x32_bf16 v[226:229], v[122:125], v[58:61], v[226:229]\n\tv_add_f32_dpp v243, v250, v243 wave_ror:1 row_mask:0xf bank_mask:0xf bound_ctrl:1\n\tv_add_f32_dpp v247, v251, v247 wave_ror:1 row_mask:0xf bank_mask:0xf bound_ctrl:1\n\t" \
  "v_mfma_f32_16x16x32_bf16 v[230:233], v[126:129], v[58:61], v[230:233]\n\tv_mfma_f32_16x16x32_bf16 v[226:229], v[114:117], v[62:65], v[226:229]\n\tv_cndmask_b32_e32 v250, v243, v239, vcc\n\t" \
  "v_cndmask_b32_e32 v251, v247, v243, vcc\n\tv_mfma_f32_16x16x32_bf16 v[230:233], v[118:121], v[62:65], v[230:233]\n\tv_mfma_f32_16x16x32_bf16 v[226:229], v[106:109], v[66:69], v[226:229]\n\t" \
  "v_mfma_f32_16x16x32_bf16 v[230:233], v[110:113], v[66:69], v[230:233]\n\tv_add_f32_dpp v242, v250, v242 wave_ror:1 row_mask:0xf bank_mask:0xf bound_ctrl:1\n\tv_add_f32_dpp v246, v251, v246 wave_ror:1 row_mask:0xf bank_mask:0xf bound_ctrl:1\n\t" \
  "s_waitcnt lgkmcnt(15)\n\tv_mfma_f32_16x16x32_bf16 v[226:229], v[170:173], v[34:37], v[226:229]\n\tv_mfma_f32_16x16x32_bf16 v[230:233], v[174:177], v[34:37], v[230:233]\n\t" \
  "v_cvt_pk_bf16_f32 v252, v242, v246\n\tv_mfma_f32_16x16x32_bf16 v[226:229], v[98:101], v[70:73], v[226:229]\n\tv_mfma_f32_16x16x32_bf16 v[230:233], v[102:105], v[70:73], v[230:233]\n\t" \
  "v_mfma_f32_16x16x32_bf16 v[234:237], v[170:173], v[38:41], 0\n\tds_read_b64_tr_b16 v[86:87], %8 offset:3328\n\tds_read_b64_tr_b16 v[88:89], %8 offset:3360\n\t" \
  "v_mfma_f32_16x16x32_bf16 v[238:241], v[174:177], v[38:41], 0\n\tds_read_b64_tr_b16 v[202:203], %4 offset:13312\n\tds_read_b64_tr_b16 v[204:205], %5 offset:13312\n\t" \
  "v_mfma_f32_16x16x32_bf16 v[234:237], v[162:165], v[42:45], v[234:237]\n\tds_read_b64_tr_b16 v[206:207], %6 offset:13312\n\tds_read_b64_tr_b16 v[208:209], %7 offset:13312\n\t" \
  "v_mfma_f32_16x16x32_bf16 v[238:241], v[166:169], v[42:45], v[238:241]\n\tv_cndmask_b32_e32 v250, v229, v249, vcc\n\tv_cndmask_b32_e32 v251, v233, v229, vcc\n\t" \
  "v_mfma_f32_16x16x32_bf16 v[234:237], v[154:157], v[46:49], v[234:237]\n\tv_mfma_f32_16x16x32_bf16 v[238:241], v[158:161], v[46:49], v[238:241]\n\tv_add_f32_dpp v228, v250, v228 wave_ror:1 row_mask:0xf bank_mask:0xf bound_ctrl:1\n\t" \
  "v_add_f32_dpp v232, v251, v232 wave_ror:1 row_mask:0xf bank_mask:0xf bound_ctrl:1\n\tv_mfma_f32_16x16x32_bf16 v[234:237], v[146:149], v[50:53], v[234:237]\n\tv_mfma_f32_16x16x32_bf16 v[238:241], v[150:153], v[50:53], v[238:241]\n\t" \
  "v_mfma_f32_16x16x32_bf16 v[234:237], v[138:141], v[54:57], v[234:237]\n\tv_cndmask_b32_e32 v250, v228, v248, vcc\n\tv_cndmask_b32_e32 v251, v232, v228, vcc\n\t" \
  "v_mfma_f32_16x16x32_bf16 v[238:241], v[142:145], v[54:57], v[238:241]\n\tv_mfma_f32_16x16x32_bf16 v[234:237], v[130:133], v[58:61], v[234:237]\n\tv_mfma_f32_16x16x32_bf16 v[238:241], v[134:137], v[58:61], v[238:241]\n\t" \
  "v_add_f32_dpp v227, v250, v227 wave_ror:1 row_mask:0xf bank_mask:0xf bound_ctrl:1\n\tv_add_f32_dpp v231, v251, v231 wave_ror:1 row_mask:0xf bank_mask:0xf bound_ctrl:1\n\tv_mfma_f32_16x16x32_bf16 v[234:237], v[122:125], v[62:65], v[234:237]\n\t" \
  "v_mfma_f32_16x16x32_bf16 v[238:241], v[126:129], v[62:65], v[238:241]\n\tv_cndmask_b32_e32 v250, v227, v247, vcc\n\tv_cndmask_b32_e32 v251, v231, v227, vcc\n\t" \
  "v_mfma_f32_16x16x32_bf16 v[234:237], v[114:117], v[66:69], v[234:237]\n\tv_mfma_f32_16x16x32_bf16 v[238:241], v[118:121], v[66:69], v[238:241]\n\tv_mfma_f32_16x16x32_bf16 v[234:237], v[106:109], v[70:73], v[234:237]\n\t" \
  "v_add_f32_dpp v226, v250, v226 wave_ror:1 row_mask:0xf bank_mask:0xf bound_ctrl:1\n\tv_add_f32_dpp v230, v251, v230 wave_ror:1 row_mask:0xf bank_mask:0xf bound_ctrl:1\n\tv_mfma_f32_16x16x32_bf16 v[238:241], v[110:113], v[70:73], v[238:241]\n\t" \
  "s_waitcnt lgkmcnt(15)\n\tv_mfma_f32_16x16x32_bf16 v[234:237], v[178:181], v[34:37], v[234:237]\n\tv_mfma_f32_16x16x32_bf16 v[238:241], v[182:185], v[34:37], v[238:241]\n\t" \
  "v_cvt_pk_bf16_f32 v253, v226, v230\n\tv_mfma_f32_16x16x32_bf16 v[234:237], v[98:101], v[74:77], v[234:237]\n\tv_mfma_f32_16x16x32_bf16 v[238:241], v[102:105], v[74:77], v[238:241]\n\t" \
  "v_mfma_f32_16x16x32_bf16 v[242:245], v[178:181], v[38:41], 0\n\tds_read_b64_tr_b16 v[90:91], %8 offset:3584\n\tds_read_b64_tr_b16 v[92:93], %8 offset:3616\n\t" \
  "v_mfma_f32_16x16x32_bf16 v[246:249], v[182:185], v[38:41], 0\n\tds_read_b64_tr_b16 v[210:211], %4 offset:14336\n\tds_read_b64_tr_b16 v[212:213], %5 offset:14336\n\t" \
  "v_mfma_f32_16x16x32_bf16 v[242:245], v[170:173], v[42:45], v[242:245]\n\tds_read_b64_tr_b16 v[214:215], %6 offset:14336\n\tds_read_b64_tr_b16 v[216:217], %7 offset:14336\n\t" \
  "v_mfma_f32_16x16x32_bf16 v[246:249], v[174:177], v[42:45], v[246:249]\n\tv_cndmask_b32_e32 v250, v237, v233, vcc\n\tv_cndmask_b32_e32 v251, v241, v237, vcc\n\t" \
  "v_mfma_f32_16x16x32_bf16 v[242:245], v[162:165], v[46:49], v[242:245]\n\tv_mfma_f32_16x16x32_bf16 v[246:249], v[166:169], v[46:49], v[246:249]\n\tv_mfma_f32_16x16x32_bf16 v[242:245], v[154:157], v[50:53], v[242:245]\n\t" \
  "v_add_f32_dpp v236, v250, v236 wave_ror:1 row_mask:0xf bank_mask:0xf bound_ctrl:1\n\tv_add_f32_dpp v240, v251, v240 wave_ror:1 row_mask:0xf bank_mask:0xf bound_ctrl:1\n\tv_mfma_f32_16x16x32_bf16 v[246:249], v[158:161], v[50:53], v[246:249]\n\t" \
  "v_mfma_f32_16x16x32_bf16 v[242:245], v[146:149], v[54:57], v[242:245]\n\tv_mfma_f32_16x16x32_bf16 v[246:249], v[150:153], v[54:57], v[246:249]\n\tv_cndmask_b32_e32 v250, v236, v232, vcc\n\t" \
  "v_cndmask_b32_e32 v251, v240, v236, vcc\n\tv_mfma_f32_16x16x32_bf16 v[242:245], v[138:141], v[58:61], v[242:245]\n\tv_mfma_f32_16x16x32_bf16 v[246:249], v[142:145], v[58:61], v[246:249]\n\t" \
  "v_mfma_f32_16x16x32_bf16 v[242:245], v[130:133], v[62:65], v[242:245]\n\tv_add_f32_dpp v235, v250, v235 wave_ror:1 row_mask:0xf bank_mask:0xf bound_ctrl:1\n\tv_add_f32_dpp v239, v251, v239 wave_ror:1 row_mask:0xf bank_mask:0xf bound_ctrl:1\n\t" \
  "v_mfma_f32_16x16x32_bf16 v[246:249], v[134:137], v[62:65], v[246:249]\n\tv_mfma_f32_16x16x32_bf16 v[242:245], v[122:125], v[66:69], v[242:245]\n\tv_mfma_f32_16x16x32_bf16 v[246:249], v[126:129], v[66:69], v[246:249]\n\t" \
  "v_cndmask_b32_e32 v250, v235, v231, vcc\n\tv_cndmask_b32_e32 v251, v239, v235, vcc\n\tv_mfma_f32_16x16x32_bf16 v[242:245], v[114:117], v[70:73], v[242:245]\n\t" \
  "v_mfma_f32_16x16x32_bf16 v[246:249], v[118:121], v[70:73], v[246:249]\n\tv_mfma_f32_16x16x32_bf16 v[242:245], v[106:109], v[74:77], v[242:245]\n\tv_add_f32_dpp v234, v250, v234 wave_ror:1 row_mask:0xf bank_mask:0xf bound_ctrl:1\n\t" \
  "v_add_f32_dpp v238, v251, v238 wave_ror:1 row_mask:0xf bank_mask:0xf bound_ctrl:1\n\tv_mfma_f32_16x16x32_bf16 v[246:249], v[110:113], v[74:77], v[246:249]\n\ts_waitcnt lgkmcnt(15)\n\t" \
  "v_mfma_f32_16x16x32_bf16 v[242:245], v[186:189], v[34:37], v[242:245]\n\tv_mfma_f32_16x16x32_bf16 v[246:249], v[190:193], v[34:37], v[246:249]\n\tv_cvt_pk_bf16_f32 v254, v234, v238\n\t" \
  "v_mfma_f32_16x16x32_bf16 v[242:245], v[98:101], v[78:81], v[242:245]\n\tv_mfma_f32_16x16x32_bf16 v[246:249], v[102:105], v[78:81], v[246:249]\n\tv_mfma_f32_16x16x32_bf16 v[226:229], v[186:189], v[38:41], 0\n\t" \
  "ds_read_b64_tr_b16 v[94:95], %8 offset:3840\n\tds_read_b64_tr_b16 v[96:97], %8 offset:3872\n\tv_mfma_f32_16x16x32_bf16 v[230:233], v[190:193], v[38:41], 0\n\t" \
  "ds_read_b64_tr_b16 v[218:219], %4 offset:15360\n\tds_read_b64_tr_b16 v[220:221], %5 offset:15360\n\tv_mfma_f32_16x16x32_bf16 v[226:229], v[178:181], v[42:45], v[226:229]\n\t" \
  "ds_read_b64_tr_b16 v[222:223], %6 offset:15360\n\tds_read_b64_tr_b16 v[224:225], %7 offset:15360\n\tv_mfma_f32_16x16x32_bf16 v[230:233], v[182:185], v[42:45], v[230:233]\n\t" \
  "v_mov_b32 v250, 1\n\ts_mov_b64 exec, 1\n\tds_write_b32 %12, v250 offset:48\n\t" \
  "s_mov_b64 exec, -1\n\tv_mfma_f32_16x16x32_bf16 v[226:229], v[170:173], v[46:49], v[226:229]\n\tv_cndmask_b32_e32 v250, v245, v241, vcc\n\t" \
  "v_cndmask_b32_e32 v251, v249, v245, vcc\n\tv_mfma_f32_16x16x32_bf16 v[230:233], v[174:177], v[46:49], v[230:233]\n\tv_mfma_f32_16x16x32_bf16 v[226:229], v[162:165], v[50:53], v[226:229]\n\t" \
  "v_mfma_f32_16x16x32_bf16 v[230:233], v[166:169], v[50:53], v[230:233]\n\tv_add_f32_dpp v244, v250, v244 wave_ror:1 row_mask:0xf bank_mask:0xf bound_ctrl:1\n\tv_add_f32_dpp v248, v251, v248 wave_ror:1 row_mask:0xf bank_mask:0xf bound_ctrl:1\n\t" \
  "v_mfma_f32_16x16x32_bf16 v[226:229], v[154:157], v[54:57], v[226:229]\n\tv_mfma_f32_16x16x32_bf16 v[230:233], v[158:161], v[54:57], v[230:233]\n\tv_mfma_f32_16x16x32_bf16 v[226:229], v[146:149], v[58:61], v[226:229]\n\t" \
  "v_cndmask_b32_e32 v250, v244, v240, vcc\n\tv_cndmask_b32_e32 v251, v248, v244, vcc\n\tv_mfma_f32_16x16x32_bf16 v[230:233], v[150:153], v[58:61], v[230:233]\n\t" \
  "v_mfma_f32_16x16x32_bf16 v[226:229], v[138:141], v[62:65], v[226:229]\n\tv_mfma_f32_16x16x32_bf16 v[230:233], v[142:145], v[62:65], v[230:233]\n\tv_add_f32_dpp v243, v250, v243 wave_ror:1 row_mask:0xf bank_mask:0xf bound_ctrl:1\n\t" \
  "v_add_f32_dpp v247, v251, v247 wave_ror:1 row_mask:0xf bank_mask:0xf bound_ctrl:1\n\tv_mfma_f32_16x16x32_bf16 v[226:229], v[130:133], v[66:69], v[226:229]\n\tv_mfma_f32_16x16x32_bf16 v[230:233], v[134:137], v[66:69], v[230:233]\n\t" \
  "v_mfma_f32_16x16x32_bf16 v[226:229], v[122:125], v[70:73], v[226:229]\n\tv_cndmask_b32_e32 v250, v243, v239, vcc\n\tv_cndmask_b32_e32 v251, v247, v243, vcc\n\t" \
  "v_mfma_f32_16x16x32_bf16 v[230:233], v[126:129], v[70:73], v[230:233]\n\tv_mfma_f32_16x16x32_bf16 v[226:229], v[114:117], v[74:77], v[226:229]\n\tv_mfma_f32_16x16x32_bf16 v[230:233], v[118:121], v[74:77], v[230:233]\n\t" \
  "v_add_f32_dpp v242, v250, v242 wave_ror:1 row_mask:0xf bank_mask:0xf bound_ctrl:1\n\tv_add_f32_dpp v246, v251, v246 wave_ror:1 row_mask:0xf bank_mask:0xf bound_ctrl:1\n\tv_mfma_f32_16x16x32_bf16 v[226:229], v[106:109], v[78:81], v[226:229]\n\t" \
  "v_mfma_f32_16x16x32_bf16 v[230:233], v[110:113], v[78:81], v[230:233]\n\ts_waitcnt lgkmcnt(15)\n\tv_mfma_f32_16x16x32_bf16 v[226:229], v[194:197], v[34:37], v[226:229]\n\t" \
  "v_cvt_pk_bf16_f32 v255, v242, v246\n\tbuffer_store_dwordx4 v[252:255], %9, %13, 0 offen offset:2048 sc1\n\tv_mfma_f32_16x16x32_bf16 v[230:233], v[198:201], v[34:37], v[230:233]\n\t" \
  "v_mfma_f32_16x16x32_bf16 v[226:229], v[98:101], v[82:85], v[226:229]\n\tv_mfma_f32_16x16x32_bf16 v[230:233], v[102:105], v[82:85], v[230:233]\n\tv_mfma_f32_16x16x32_bf16 v[234:237], v[194:197], v[38:41], 0\n\t" \
  "v_mfma_f32_16x16x32_bf16 v[238:241], v[198:201], v[38:41], 0\n\tv_mfma_f32_16x16x32_bf16 v[234:237], v[186:189], v[42:45], v[234:237]\n\tv_mfma_f32_16x16x32_bf16 v[238:241], v[190:193], v[42:45], v[238:241]\n\t" \
  "s_nop 2\n\tv_cndmask_b32_e32 v250, v229, v249, vcc\n\tv_cndmask_b32_e32 v251, v233, v229, vcc\n\t" \
  "v_mfma_f32_16x16x32_bf16 v[234:237], v[178:181], v[46:49], v[234:237]\n\tv_mfma_f32_16x16x32_bf16 v[238:241], v[182:185], v[46:49], v[238:241]\n\tv_mfma_f32_16x16x32_bf16 v[234:237], v[170:173], v[50:53], v[234:237]\n\t" \
  "v_add_f32_dpp v228, v250, v228 wave_ror:1 row_mask:0xf bank_mask:0xf bound_ctrl:1\n\tv_add_f32_dpp v232, v251, v232 wave_ror:1 row_mask:0xf bank_mask:0xf bound_ctrl:1\n\tv_mfma_f32_16x16x32_bf16 v[238:241], v[174:177], v[50:53], v[238:241]\n\t" \
  "v_mfma_f32_16x16x32_bf16 v[234:237], v[162:165], v[54:57], v[234:237]\n\tv_mfma_f32_16x16x32_bf16 v[238:241], v[166:169], v[54:57], v[238:241]\n\tv_mfma_f32_16x16x32_bf16 v[234:237], v[154:157], v[58:61], v[234:237]\n\t" \
  "v_cndmask_b32_e32 v250, v228, v248, vcc\n\tv_cndmask_b32_e32 v251, v232, v228, vcc\n\tv_mfma_f32_16x16x32_bf16 v[238:241], v[158:161], v[58:61], v[238:241]\n\t" \
  "v_mfma_f32_16x16x32_bf16 v[234:237], v[146:149], v[62:65], v[234:237]\n\tv_mfma_f32_16x16x32_bf16 v[238:241], v[150:153], v[62:65], v[238:241]\n\tv_add_f32_dpp v227, v250, v227 wave_ror:1 row_mask:0xf bank_mask:0xf bound_ctrl:1\n\t" \
  "v_add_f32_dpp v231, v251, v231 wave_ror:1 row_mask:0xf bank_mask:0xf bound_ctrl:1\n\tv_mfma_f32_16x16x32_bf16 v[234:237], v[138:141], v[66:69], v[234:237]\n\tv_mfma_f32_16x16x32_bf16 v[238:241], v[142:145], v[66:69], v[238:241]\n\t" \
  "v_mfma_f32_16x16x32_bf16 v[234:237], v[130:133], v[70:73], v[234:237]\n\tv_mfma_f32_16x16x32_bf16 v[238:241], v[134:137], v[70:73], v[238:241]\n\tv_cndmask_b32_e32 v250, v227, v247, vcc\n\t" \
  "v_cndmask_b32_e32 v251, v231, v227, vcc\n\tv_mfma_f32_16x16x32_bf16 v[234:237], v[122:125], v[74:77], v[234:237]\n\tv_mfma_f32_16x16x32_bf16 v[238:241], v[126:129], v[74:77], v[238:241]\n\t" \
  "v_mfma_f32_16x16x32_bf16 v[234:237], v[114:117], v[78:81], v[234:237]\n\tv_add_f32_dpp v226, v250, v226 wave_ror:1 row_mask:0xf bank_mask:0xf bound_ctrl:1\n\tv_add_f32_dpp v230, v251, v230 wave_ror:1 row_mask:0xf bank_mask:0xf bound_ctrl:1\n\t" \
  "v_mfma_f32_16x16x32_bf16 v[238:241], v[118:121], v[78:81], v[238:241]\n\tv_mfma_f32_16x16x32_bf16 v[234:237], v[106:109], v[82:85], v[234:237]\n\tv_mfma_f32_16x16x32_bf16 v[238:241], v[110:113], v[82:85], v[238:241]\n\t" \
  "v_mfma_f32_16x16x32_bf16 v[234:237], v[202:205], v[34:37], v[234:237]\n\tv_cvt_pk_bf16_f32 v252, v226, v230\n\ts_waitcnt lgkmcnt(13)\n\t" \
  "v_mfma_f32_16x16x32_bf16 v[238:241], v[206:209], v[34:37], v[238:241]\n\tv_mfma_f32_16x16x32_bf16 v[234:237], v[98:101], v[86:89], v[234:237]\n\tv_mfma_f32_16x16x32_bf16 v[238:241], v[102:105], v[86:89], v[238:241]\n\t" \
  "v_mfma_f32_16x16x32_bf16 v[242:245], v[202:205], v[38:41], 0\n\tv_mfma_f32_16x16x32_bf16 v[246:249], v[206:209], v[38:41], 0\n\tv_mfma_f32_16x16x32_bf16 v[242:245], v[194:197], v[42:45], v[242:245]\n\t" \
  "v_mfma_f32_16x16x32_bf16 v[246:249], v[198:201], v[42:45], v[246:249]\n\ts_nop 2\n\tv_cndmask_b32_e32 v250, v237, v233, vcc\n\t" \
  "v_cndmask_b32_e32 v251, v241, v237, vcc\n\tv_mfma_f32_16x16x32_bf16 v[242:245], v[186:189], v[46:49], v[242:245]\n\tv_mfma_f32_16x16x32_bf16 v[246:249], v[190:193], v[46:49], v[246:249]\n\t" \
  "v_mfma_f32_16x16x32_bf16 v[242:245], v[178:181], v[50:53], v[242:245]\n\tv_add_f32_dpp v236, v250, v236 wave_ror:1 row_mask:0xf bank_mask:0xf bound_ctrl:1\n\tv_add_f32_dpp v240, v251, v240 wave_ror:1 row_mask:0xf bank_mask:0xf bound_ctrl:1\n\t" \
  "v_mfma_f32_16x16x32_bf16 v[246:249], v[182:185], v[50:53], v[246:249]\n\tv_mfma_f32_16x16x32_bf16 v[242:245], v[170:173], v[54:57], v[242:245]\n\tv_mfma_f32_16x16x32_bf16 v[246:249], v[174:177], v[54:57], v[246:249]\n\t" \
  "v_mfma_f32_16x16x32_bf16 v[242:245], v[162:165], v[58:61], v[242:245]\n\tv_cndmask_b32_e32 v250, v236, v232, vcc\n\tv_cndmask_b32_e32 v251, v240, v236, vcc\n\t" \
  "v_mfma_f32_16x16x32_bf16 v[246:249], v[166:169], v[58:61], v[246:249]\n\tv_mfma_f32_16x16x32_bf16 v[242:245], v[154:157], v[62:65], v[242:245]\n\tv_mfma_f32_16x16x32_bf16 v[246:249], v[158:161], v[62:65], v[246:249]\n\t" \
  "v_mfma_f32_16x16x32_bf16 v[242:245], v[146:149], v[66:69], v[242:245]\n\tv_add_f32_dpp v235, v250, v235 wave_ror:1 row_mask:0xf bank_mask:0xf bound_ctrl:1\n\tv_add_f32_dpp v239, v251, v239 wave_ror:1 row_mask:0xf bank_mask:0xf bound_ctrl:1\n\t" \
  "v_mfma_f32_16x16x32_bf16 v[246:249], v[150:153], v[66:69], v[246:249]\n\tv_mfma_f32_16x16x32_bf16 v[242:245], v[138:141], v[70:73], v[242:245]\n\tv_mfma_f32_16x16x32_bf16 v[246:249], v[142:145], v[70:73], v[246:249]\n\t" \
  "v_mfma_f32_16x16x32_bf16 v[242:245], v[130:133], v[74:77], v[242:245]\n\tv_cndmask_b32_e32 v250, v235, v231, vcc\n\tv_cndmask_b32_e32 v251, v239, v235, vcc\n\t" \
  "v_mfma_f32_16x16x32_bf16 v[246:249], v[134:137], v[74:77], v[246:249]\n\tv_mfma_f32_16x16x32_bf16 v[242:245], v[122:125], v[78:81], v[242:245]\n\tv_mfma_f32_16x16x32_bf16 v[246:249], v[126:129], v[78:81], v[246:249]\n\t" \
  "v_mfma_f32_16x16x32_bf16 v[242:245], v[114:117], v[82:85], v[242:245]\n\tv_add_f32_dpp v234, v250, v234 wave_ror:1 row_mask:0xf bank_mask:0xf bound_ctrl:1\n\tv_add_f32_dpp v238, v251, v238 wave_ror:1 row_mask:0xf bank_mask:0xf bound_ctrl:1\n\t" \
  "v_mfma_f32_16x16x32_bf16 v[246:249], v[118:121], v[82:85], v[246:249]\n\tv_mfma_f32_16x16x32_bf16 v[242:245], v[106:109], v[86:89], v[242:245]\n\tv_mfma_f32_16x16x32_bf16 v[246:249], v[110:113], v[86:89], v[246:249]\n\t" \
  "s_waitcnt lgkmcnt(9)\n\tv_mfma_f32_16x16x32_bf16 v[242:245], v[210:213], v[34:37], v[242:245]\n\tv_cvt_pk_bf16_f32 v253, v234, v238\n\t" \
  "s_waitcnt lgkmcnt(7)\n\tv_mfma_f32_16x16x32_bf16 v[246:249], v[214:217], v[34:37], v[246:249]\n\tv_mfma_f32_16x16x32_bf16 v[242:245], v[98:101], v[90:93], v[242:245]\n\t" \
  "v_mfma_f32_16x16x32_bf16 v[246:249], v[102:105], v[90:93], v[246:249]\n\tv_mfma_f32_16x16x32_bf16 v[226:229], v[210:213], v[38:41], 0\n\tv_mfma_f32_16x16x32_bf16 v[230:233], v[214:217], v[38:41], 0\n\t" \
  "v_mfma_f32_16x16x32_bf16 v[226:229], v[202:205], v[42:45], v[226:229]\n\tv_mfma_f32_16x16x32_bf16 v[230:233], v[206:209], v[42:45], v[230:233]\n\ts_nop 2\n\t" \
  "v_cndmask_b32_e32 v250, v245, v241, vcc\n\tv_cndmask_b32_e32 v251, v249, v245, vcc\n\tv_mfma_f32_16x16x32_bf16 v[226:229], v[194:197], v[46:49], v[226:229]\n\t" \
  "v_mfma_f32_16x16x32_bf16 v[230:233], v[198:201], v[46:49], v[230:233]\n\tv_mfma_f32_16x16x32_bf16 v[226:229], v[186:189], v[50:53], v[226:229]\n\tv_mfma_f32_16x16x32_bf16 v[230:233], v[190:193], v[50:53], v[230:233]\n\t" \
  "v_add_f32_dpp v244, v250, v244 wave_ror:1 row_mask:0xf bank_mask:0xf bound_ctrl:1\n\tv_add_f32_dpp v248, v251, v248 wave_ror:1 row_mask:0xf bank_mask:0xf bound_ctrl:1\n\tv_mfma_f32_16x16x32_bf16 v[226:229], v[178:181], v[54:57], v[226:229]\n\t" \
  "v_mfma_f32_16x16x32_bf16 v[230:233], v[182:185], v[54:57], v[230:233]\n\tv_mfma_f32_16x16x32_bf16 v[226:229], v[170:173], v[58:61], v[226:229]\n\tv_mfma_f32_16x16x32_bf16 v[230:233], v[174:177], v[58:61], v[230:233]\n\t" \
  "v_cndmask_b32_e32 v250, v244, v240, vcc\n\tv_cndmask_b32_e32 v251, v248, v244, vcc\n\tv_mfma_f32_16x16x32_bf16 v[226:229], v[162:165], v[62:65], v[226:229]\n\t" \
  "v_mfma_f32_16x16x32_bf16 v[230:233], v[166:169], v[62:65], v[230:233]\n\tv_mfma_f32_16x16x32_bf16 v[226:229], v[154:157], v[66:69], v[226:229]\n\tv_mfma_f32_16x16x32_bf16 v[230:233], v[158:161], v[66:69], v[230:233]\n\t" \
  "v_add_f32_dpp v243, v250, v243 wave_ror:1 row_mask:0xf bank_mask:0xf bound_ctrl:1\n\tv_add_f32_dpp v247, v251, v247 wave_ror:1 row_mask:0xf bank_mask:0xf bound_ctrl:1\n\tv_mfma_f32_16x16x32_bf16 v[226:229], v[146:149], v[70:73], v[226:229]\n\t" \
  "v_mfma_f32_16x16x32_bf16 v[230:233], v[150:153], v[70:73], v[230:233]\n\tv_mfma_f32_16x16x32_bf16 v[226:229], v[138:141], v[74:77], v[226:229]\n\tv_mfma_f32_16x16x32_bf16 v[230:233], v[142:145], v[74:77], v[230:233]\n\t" \
  "v_cndmask_b32_e32 v250, v243, v239, vcc\n\tv_cndmask_b32_e32 v251, v247, v243, vcc\n\tv_mfma_f32_16x16x32_bf16 v[226:229], v[130:133], v[78:81], v[226:229]\n\t" \
  "v_mfma_f32_16x16x32_bf16 v[230:233], v[134:137], v[78:81], v[230:233]\n\tv_mfma_f32_16x16x32_bf16 v[226:229], v[122:125], v[82:85], v[226:229]\n\tv_mfma_f32_16x16x32_bf16 v[230:233], v[126:129], v[82:85], v[230:233]\n\t" \
  "v_add_f32_dpp v242, v250, v242 wave_ror:1 row_mask:0xf bank_mask:0xf bound_ctrl:1\n\tv_add_f32_dpp v246, v251, v246 wave_ror:1 row_mask:0xf bank_mask:0xf bound_ctrl:1\n\tv_mfma_f32_16x16x32_bf16 v[226:229], v[114:117], v[86:89], v[226:229]\n\t" \
  "v_mfma_f32_16x16x32_bf16 v[230:233], v[118:121], v[86:89], v[230:233]\n\tv_mfma_f32_16x16x32_bf16 v[226:229], v[106:109], v[90:93], v[226:229]\n\tv_mfma_f32_16x16x32_bf16 v[230:233], v[110:113], v[90:93], v[230:233]\n\t" \
  "v_cvt_pk_bf16_f32 v254, v242, v246\n\ts_waitcnt lgkmcnt(3)\n\tv_mfma_f32_16x16x32_bf16 v[226:229], v[218:221], v[34:37], v[226:229]\n\t" \
  "s_waitcnt lgkmcnt(1)\n\tv_mfma_f32_16x16x32_bf16 v[230:233], v[222:225], v[34:37], v[230:233]\n\tv_mfma_f32_16x16x32_bf16 v[226:229], v[98:101], v[94:97], v[226:229]\n\t" \
  "v_mfma_f32_16x16x32_bf16 v[230:233], v[102:105], v[94:97], v[230:233]\n\ts_nop 7\n\tv_cndmask_b32_e32 v250, v229, v249, vcc\n\t" \
  "v_cndmask_b32_e32 v251, v233, v229, vcc\n\ts_nop 0\n\tv_add_f32_dpp v228, v250, v228 wave_ror:1 row_mask:0xf bank_mask:0xf bound_ctrl:1\n\t" \
  "v_add_f32_dpp v232, v251, v232 wave_ror:1 row_mask:0xf bank_mask:0xf bound_ctrl:1\n\tv_cndmask_b32_e32 v250, v228, v248, vcc\n\tv_cndmask_b32_e32 v251, v232, v228, vcc\n\t" \
  "s_nop 0\n\tv_add_f32_dpp v227, v250, v227 wave_ror:1 row_mask:0xf bank_mask:0xf bound_ctrl:1\n\tv_add_f32_dpp v231, v251, v231 wave_ror:1 row_mask:0xf bank_mask:0xf bound_ctrl:1\n\t" \
  "v_cndmask_b32_e32 v250, v227, v247, vcc\n\tv_cndmask_b32_e32 v251, v231, v227, vcc\n\ts_nop 0\n\t" \
  "v_add_f32_dpp v226, v250, v226 wave_ror:1 row_mask:0xf bank_mask:0xf bound_ctrl:1\n\tv_add_f32_dpp v230, v251, v230 wave_ror:1 row_mask:0xf bank_mask:0xf bound_ctrl:1\n\tv_cvt_pk_bf16_f32 v255, v226, v230\n\t" \
  "buffer_store_dwordx4 v[252:255], %9, %13, 0 offen offset:3072 sc1\n\ts_nop 1\n\t"
#define CONV_CLOBBERS "v34", "v35", "v36", "v37", "v38", "v39", "v40", "v41", "v42", "v43", "v44", "v45", "v46", "v47", "v48", "v49", "v50", "v51", "v52", "v53", "v54", "v55", "v56", "v57", "v58", "v59", "v60", "v61", "v62", "v63", "v64", "v65", "v66", "v67", "v68", "v69", "v70", "v71", "v72", "v73", "v74", "v75", "v76", "v77", "v78", "v79", "v80", "v81", "v82", "v83", "v84", "v85", "v86", "v87", "v88", "v89", "v90", "v91", "v92", "v93", "v94", "v95", "v96", "v97", "v98", "v99", "v100", "v101", "v102", "v103", "v104", "v105", "v106", "v107", "v108", "v109", "v110", "v111", "v112", "v113", "v114", "v115", "v116", "v117", "v118", "v119", "v120", "v121", "v122", "v123", "v124", "v125", "v126", "v127", "v128", "v129", "v130", "v131", "v132", "v133", "v134", "v135", "v136", "v137", "v138", "v139", "v140", "v141", "v142", "v143", "v144", "v145", "v146", "v147", "v148", "v149", "v150", "v151", "v152", "v153", "v154", "v155", "v156", "v157", "v158", "v159", "v160", "v161", "v162", "v163", "v164", "v165", "v166", "v167", "v168", "v169", "v170", "v171", "v172", "v173", "v174", "v175", "v176", "v177", "v178", "v179", "v180", "v181", "v182", "v183", "v184", "v185", "v186", "v187", "v188", "v189", "v190", "v191", "v192", "v193", "v194", "v195", "v196", "v197", "v198", "v199", "v200", "v201", "v202", "v203", "v204", "v205", "v206", "v207", "v208", "v209", "v210", "v211", "v212", "v213", "v214", "v215", "v216", "v217", "v218", "v219", "v220", "v221", "v222", "v223", "v224", "v225", "v226", "v227", "v228", "v229", "v230", "v231", "v232", "v233", "v234", "v235", "v236", "v237", "v238", "v239", "v240", "v241", "v242", "v243", "v244", "v245", "v246", "v247", "v248", "v249", "v250", "v251", "v252", "v253", "v254", "v255"
#define G1_K0 12
#define A1_START 3
#define A1_PER 23
#define A1_LAG 2
#define A1_STAG 3
#define A1_ROWS 11
#define A1_DLY 8
#define H_AT 10
#define Q_AT 11
#define T_AT 99
#define A_LAG 0
#define XQ_AT 16
#define XQ_LAG 1
#define MK_AT 16
#define X_LAG 1
#define NTOUCH 3
#define TOUCH_DLY 6
#define PF_ON 0
#define LORD 0
#define PFD 0
#define BAR16_AT -1
#define WH_AT 8
#define GATE F_X1
#define ROTM 11
#define YSLEEP 6
__device__ __forceinline__ void helper_wave(const Params& P, lptr smem, int hw, int lane, int xl, int idx) {
  PAD_DECL(1)
  constexpr int LAST_ = 9;
  ST_DECL
  STAMP(0);
  const int hp = (xl & 3) * 4 + (idx & 3), rb = (xl >> 2) * 8 + (idx >> 2);
  const int hq = hp ^ QSWAP;
  const int nt = G2MAP ? ((idx & 15) ^ NTX) : 8 * (xl & 1) + ((idx & 7) ^ NTX);
  const unsigned voW = wo_voff(lane);
  const int ch = 128 * xl + 64 * (idx >> 4) + 8 * (4 * (idx & 1) + hw) + ((idx >> 1) & 7);
  u32x4 qv[4];
  auto hburst = [&]() {
    const int i2 = 2 * (hw & 1);
    const char* hsrc = (const char*)P.ssm_k + (size_t)(128 * xl + 64 * (idx >> 4) + 8 * (4 * (idx & 1) + i2) + ((idx >> 1) & 7)) * 4096;
    const unsigned vl = lane * 16;
#pragma unroll
    for (int i = 0; i < 2; ++i) dma4(hsrc + i * 8 * 4096, (unsigned)(size_t)smem + CV_HP + (i2 + i) * CV_HS + 32, vl, vl, vl, vl);
  };
  auto x_dma = [&]() {
    const char* xs = P.ws + WS_KVT + (rb * 16 + hq) * 16384 + (hw & 1) * 8192;
    const unsigned vl = lane * 16;
#pragma unroll 1
    for (int i = 0; i < 2; ++i)
      asm volatile("s_mov_b32 m0, %0\n\ts_nop 0\n\tglobal_load_lds_dwordx4 %2, %1 sc1\n\tglobal_load_lds_dwordx4 %2, %1 offset:1024 sc1\n\t"
                   "global_load_lds_dwordx4 %2, %1 offset:2048 sc1\n\tglobal_load_lds_dwordx4 %2, %1 offset:3072 sc1"
                   ::"s"((unsigned)(size_t)smem + CV_X + (hw & 1) * 8192 + i * 4096), "s"(xs + i * 4096), "v"(vl) : "memory");
  };
  unsigned dw0 = 0, dw1 = 0;
  const int ch0 = 128 * xl + 64 * (idx >> 4) + 8 * (4 * (idx & 1) + 2 * (hw & 1)) + ((idx >> 1) & 7);
  auto load_d = [&]() {
    unsigned t0, t1;
    asm volatile("s_load_dword %0, %2, 0x0\n\ts_load_dword %1, %2, 0x10\n\ts_waitcnt lgkmcnt(0)" : "=&s"(t0), "=&s"(t1) : "s"((const char*)P.ssm_D + (ch0 >> 1) * 4) : "memory");
    dw0 = t0; dw1 = t1;
  };
  if (__builtin_expect(hw < 2, 1)) {
    const int lw = hw;
    unsigned va[4], vb[4];
    {
      const int r = lane >> 3, c = lane & 7, h = lane >> 4;
      const unsigned a0 = r * 2048 + ((c ^ h) << 4), b0 = r * 6144 + ((c ^ ((h & 1) << 1)) << 4);
#pragma unroll
      for (int j = 0; j < 4; ++j) {
        va[j] = (a0 ^ ((j & 1) << 6)) + j * (8 * 2048 - 1024);
        vb[j] = (b0 ^ ((j & 1) << 6)) + j * (8 * 6144 - 1024);
      }
    }
    const char* xa = (const char*)P.x + (rb * 128 + 64 * lw) * 2048;
    const char* wq = (const char*)P.wqkv + 32 * lw * 6144 + hq * 128;
    const char* wk = (const char*)P.wqkv + 32 * lw * 6144 + hp * 128 + 2048;
    const int ij = (idx & 3) + (idx >> 2);
    const int rot = ROTM == 0 ? 4 * (((xl & 3) + 2 * (xl >> 2) + ((idx >> 2) & 1)) & 3) : ROTM == 3 ? 8 * (ij & 1) : ROTM == 4 ? 4 * (ij & 3)
                  : ROTM == 5 ? 5 * (ij % 3) : ROTM == 6 ? 6 * (ij & 1) : ROTM == 7 ? 8 * (ij & 1) + (xl & 7) : ROTM == 8 ? 4 * (ij & 1) : ROTM == 9 ? 3 * (ij % 3)
                  : ROTM == 11 ? 4 * (((xl & 3) + 2 * (xl >> 2)) & 3) + 2 * (idx >> 2) : ROTM == 12 ? 4 * (((xl & 3) + 2 * (xl >> 2) + (idx >> 2)) & 3)
                  : ROTM == 13 ? 2 * (idx >> 2) : ROTM == 14 ? 4 * (xl & 3) + 2 * (idx >> 2) : ROTM == 15 ? 4 * (xl & 3) + 2 * (idx >> 2) + (xl >> 2)
                  : ROTM == 16 ? 4 * (((xl & 3) + 2 * (xl >> 2)) & 3) + (idx >> 2) : ROTM == 17 ? 4 * (((xl & 3) + 2 * (xl >> 2)) & 3) + 2 * ((idx >> 2) & 1) : 0;
#define LAGM 0
#define LAGN 2
    const int cls = (hp + (int)((size_t)P.wqkv >> 7)) & 7;
    const int lag = LAGM == 1 ? (cls == 3 ? LAGN : 0) : LAGM == 2 ? (cls == 3 || cls == 2 ? LAGN : 0) : LAGM == 3 ? ((idx & 3) != 0 ? LAGN : 0)
                  : LAGM == 4 ? ((idx & 3) != ((idx >> 2) & 1) ? LAGN : 0) : LAGM == 5 ? ((idx & 2) ? LAGN : 0) : 0;
    auto issue = [&](int t, int slot, auto g0_, auto g1_) {
      const int kk = (t + rot - lag) & 15;
      const unsigned sl = (unsigned)(size_t)smem + slot;
      sfor<decltype(g0_)::v, decltype(g1_)::v>([&](auto i_) {
        constexpr int ORD[3][5] = {{0, 1, 2, 3, 4}, {3, 4, 2, 0, 1}, {3, 0, 4, 1, 2}};
        constexpr int i = ORD[LORD][decltype(i_)::v];
        if constexpr (i < 2) dma4(xa + (kk * 128 + i * 32 * 2048), sl + lw * 8192 + i * 4096, va[0], va[1], va[2], va[3]);
        else dma4((i == 2 ? wq : wk) + (kk * 64 * 6144 + (i == 4 ? 2048 : 0)), sl + 16384 + (i - 2) * 8192 + lw * 4096, vb[0], vb[1], vb[2], vb[3]);
      });
    };
    TB_DECL
    issue(0, 0, IC<0>{}, IC<5>{});
    issue(1, G1_STAGE, IC<0>{}, IC<G1_K0 / 4>{});
    waitv<G1_K0>();
    TBAR(0);
    issue(1, G1_STAGE, IC<G1_K0 / 4>{}, IC<5>{});
    int slot = G1_STAGE;
#pragma unroll 1
    for (int t = 1; t < 15; ++t) {
      slot = slot + G1_STAGE == 3 * G1_STAGE ? 0 : slot + G1_STAGE;
      issue(t + 1, slot, IC<0>{}, IC<5>{});
      waitv<20>();
      TBAR(t);
    }
    if constexpr (H_AT == 10) hburst();
    {
      const unsigned st = 8 * 2048 - 1024;
      dma4((const char*)P.wo + nt * 128 + (size_t)((G2MAP ? 2 * xl + (idx >> 4) : 4 * (xl >> 1) + (idx >> 3)) * 8 + lw * 4) * 8 * 2048, (unsigned)(size_t)smem + (10 + lw * W1N) * 4096,
           voW, voW + st, voW + 2 * st, voW + 3 * st);
    }
    constexpr int NPF = 4;
    load_d();
    waitv<(H_AT == 10 ? 8 : 0) + NPF>();
    TBAR(15);
    STAMP(1);
    TB_OUT
    if constexpr (H_AT == 11) hburst();
    wo_dma(P, smem, nt, voW, lw * W1N, 2);
    {
      waitv<8>();
      if (lane < 2) {
        const unsigned d = lane ? dw1 : dw0;
        volatile LDSAS u16* h0 = (volatile LDSAS u16*)(smem + CV_HP + (2 * lw + lane) * CV_HS + 32);
        *h0 = (u16)pack2(bflo(*h0) + u2f((ch0 & 1) ? (d & 0xffff0000u) : (d << 16)), 0.f);
      }
      waitl0();
      lset(smem, F_WH + lw * 4, 1);
    }
    spin<1>([&]() { return lget(smem, F_AR) != 0; });
    wo_dma(P, smem, nt, voW, lw * W1N + 2, W1N - 2);
  } else {
    const int u = hw - 2;
    const bool leader = blockIdx.x == 0 && u == 1, follower = blockIdx.x != 0 && u == 0;
    GAS unsigned* hbp = hb_ptr(P.ws, blockIdx.x);
    unsigned h0 = 0, h1 = 0;
    if (follower) h0 = gload(hbp);
    if (u == 0) *(LDSAS u32x4*)(smem + (lane < 56 ? CV_XP + lane * 16 : CV_HP + ((lane - 56) >> 1) * CV_HS + (lane & 1) * 16)) = u32x4{0, 0, 0, 0};
    auto beat = [&]() {
      if (leader) {
#pragma unroll
        for (int i = 0; i < 4; ++i) __hip_atomic_fetch_add(hb_ptr(P.ws, i * 64 + lane), 1u, __ATOMIC_RELAXED, __HIP_MEMORY_SCOPE_AGENT);
      }
    };
    const int cg = lane & 7, rg = lane >> 3;
    const int d0 = 128 * xl + 64 * u + 8 * cg;
    const int s0 = 64 * idx + 8 * rg;
    const unsigned dump = (unsigned)(size_t)smem + X1_Z + 256 + u * 256;
    TB_DECL
    int cur = 0;
    auto advance = [&](int n) {
      sched0();
      waitl0();
#pragma unroll 1
      for (; cur < n && cur < 16; ++cur) TBAR(cur);
      sched0();
    };
#define PF2 0
    constexpr int A1_END = (A1_START > 3 || PF_ON == 0 ? A1_START : 3) + 22 / A1_PER + A1_LAG + (63 + A1_ROWS) / A1_ROWS;
    static_assert(A1_END + A1_STAG - 1 <= 16, "");
    static_assert(A1_END <= 16 && A1_END >= BEAT_AT + 3, "hand-shake order");
    const rsrc_t rt = mkrs(P.ws + WS_KVT, 4u << 20);
    u32x4 ld[23];
    unsigned ko = (s0 * 1024 + d0) * 2, kh = s0 == 0 ? 0x80000000u : ko - 3 * 2048;
    int vkv = (((cg >> 2) ^ QSWAP) + ((xl >> 2) * 8 + 4 * u) * 16 + (xl & 3) * 4) * 16384 + rg * 64 + (((cg ^ (2 * idx + (rg >> 2))) & 3) << 4) + idx * 512;
    asm volatile("" : "+v"(ko), "+v"(kh), "+v"(vkv));
#define SLOWG -1
    const int cls_ = (hp + (int)((size_t)P.wqkv >> 7)) & 7;
    advance(A1_START + (SLOWG >= 0 && (cls_ & 6) == 2 ? SLOWG : A1_STAG > 1 ? (idx + (xl >> 1)) % A1_STAG : 0));
    if (A1_DLY) __builtin_amdgcn_s_sleep(A1_DLY);
    STAMP(2);
    sfor<0, 23>([&](auto n_) {
      constexpr int n = decltype(n_)::v;
      if constexpr (n < 4) ld[n] = *(const u32x4*)(P.shift_k + d0 * 4 + n * 8);
      else if constexpr (n < 7) ld[n] = __builtin_amdgcn_raw_buffer_load_b128(mkrs(P.k, 4u << 20), kh + (n - 4) * 2048, 0, 0);
      else if constexpr (n < 15) ld[n] = *(const u32x4*)((const char*)P.k + (n - 7) * 2048 + ko);
      else ld[n] = *(const u32x4*)((const char*)P.v + (n - 15) * 2048 + ko);
      if constexpr (n % A1_PER == A1_PER - 1 && n != 22) {
        advance(cur + 1);
        if (A1_DLY) __builtin_amdgcn_s_sleep(A1_DLY);
      }
    });
    auto ytouch = [&]() {
      const int c2 = 128 * xl + 64 * (idx >> 4) + 8 * (4 * (idx & 1) + 2 * u + (lane >> 5)) + ((idx >> 1) & 7);
      touch(P.ws + WS_Y2T, dump, c2 * 4096 + (lane & 31) * 128);
    };
    auto tburst = [&]() {
      if (lane < 32) touch(P.wo, dump, (blockIdx.x * 64 + u * 32 + lane) * 128);
    };
    if constexpr (H_AT < 0) hburst();
    if constexpr (T_AT < 0) tburst();
    advance(cur + A1_LAG);
    sfor<0, 8>([&](auto c_) {
      constexpr int c = decltype(c_)::v;
      const unsigned wa = ld[c >> 1][(c & 1) * 2], wb = ld[c >> 1][(c & 1) * 2 + 1];
      const float w0 = bflo(wa), w1 = bfhi(wa), w2 = bflo(wb), w3 = bfhi(wb);
      float kf[11], r[8];
#pragma unroll
      for (int i = 0; i < 11; ++i) {
        kf[i] = (c & 1) ? bfhi(ld[4 + i][c >> 1]) : bflo(ld[4 + i][c >> 1]);
      }
#pragma unroll
      for (int i = 0; i < 8; ++i) {
        float ks = kf[i] * w3;
        ks = __builtin_fmaf(kf[i + 1], w2, ks);
        ks = __builtin_fmaf(kf[i + 2], w1, ks);
        ks = __builtin_fmaf(kf[i + 3], w0, ks);
        r[i] = ks * ((c & 1) ? bfhi(ld[15 + i][c >> 1]) : bflo(ld[15 + i][c >> 1]));
        if ((c * 8 + i + 1) % A1_ROWS == 0 && i != 7) {
#pragma unroll
          for (int j = 0; j <= i; ++j) pin(r[j]);
          advance(cur + 1);
        }
      }
      st_sc1(u32x4{pack2(r[0], r[1]), pack2(r[2], r[3]), pack2(r[4], r[5]), pack2(r[6], r[7])}, rt, vkv, ((c >> 1) * 16 + 2 * (c & 1)) * 16384);
      if constexpr (c == 5) { if (follower) h1 = gload(hbp); }
      if constexpr (c == 7) {
        if (follower) {
          if (__builtin_amdgcn_readfirstlane(h1) == __builtin_amdgcn_readfirstlane(h0))
            spin<8>([&]() { return __builtin_amdgcn_readfirstlane(gload(hbp)) != __builtin_amdgcn_readfirstlane(h0); });
          if (lane == 0) __hip_atomic_fetch_add(ack_ptr(P.ws, xl), 1u, __ATOMIC_RELAXED, __HIP_MEMORY_SCOPE_AGENT);
        }
      }
      {
        const u32x4 mk = {MARK, MARK, MARK, MARK};
        const rsrc_t rp = mkrs(P.ws + WS_KVP, 1024 * KVP_SLOT);
        if constexpr (c == 1 || c == 3) st_sc1(mk, rp, lane * 16, ((hp * 4 + u + (c >> 1) * 2) * 16 + rb) * KVP_SLOT);
        if constexpr (c == 5) { if (lane < 16) st_sc1(mk, rp, (lane & 7) * 16 + (lane >> 3) * 32 * KVP_SLOT, ((hp * 4 + u) * 16 + rb) * KVP_SLOT + 1024); }
      }
      if constexpr (c == H_AT) hburst();
      if constexpr (c == T_AT) tburst();
      if constexpr (T_AT == 9) { if (c == (idx & 7)) tburst(); }
      if constexpr (H_AT == 9) { if (c == ((idx >> 2) & 7)) hburst(); }
      if constexpr ((c * 8 + 8) % A1_ROWS == 0 || c == 7) advance(cur + 1);
    });
    advance(cur + A_LAG);
    waitv<0>();
    gpost(P.ws, MB_A, (u * 16 + lane) * 8 + xl, 16, lane);
    if constexpr (H_AT == 8) hburst();
    if constexpr (T_AT == 8) tburst();
    STAMP(3);
    const char* abx = P.ws + WS_CNT + blockIdx.x * MB_STRIDE + MB_A * 4;
    constexpr bool xearly = false;
    if (xearly) {
      x_dma();
    }
    advance(15);
    if (!xearly && u == 0) poll_prime<0>(abx);
    advance(16);
    STAMP(1);
    TB_OUT
    if (leader) {
      GAS unsigned* ap = ack_ptr(P.ws, lane & 7);
      spin<16>([&]() {
        if (__builtin_amdgcn_ballot_w64(gload(ap) < 32u) == 0) return true;
        beat();
        return false;
      });
    }
    if (!xearly) {
      if (u == 0) poll_prime<1>(abx);
      group_wait<1, false>(smem, F_AR, u == 0, abx, 32);
      x_dma();
    } else if (u == 0) lset(smem, F_AR, 1);
    waitv<0>();
    lset(smem, F_HR + u * 4, 1);
    if constexpr (H_AT < 10) lset(smem, F_WH + u * 4, 1);
  }
  STAMP(4);
  {
    const int l15 = lane & 15, g = lane >> 4;
    const int q4 = l15 >> 2, p4 = lane & 3;
    lptr xb[2][2];
#pragma unroll
    for (int par = 0; par < 2; ++par)
#pragma unroll
      for (int hf = 0; hf < 2; ++hf) {
        const int t4 = 28 + 16 * par + 3 + 4 * p4 - 8 * g - 4 * hf - q4;
        const int s8 = t4 >> 1;
        xb[par][hf] = smem + CV_XP + s8 * 64 + ((hw ^ (((s8 + 2) >> 2) & 3)) << 4) + (t4 & 1) * 8;
      }
    lptr hb = smem + CV_HP + hw * CV_HS + 32 - 24 + 8 * p4 + 64 * g + 8 * q4;
    const rsrc_t ry = mkrs(P.ws + WS_Y2T, 4u << 20);
    int vy = ch * 4096 + lane * 16;
    asm volatile("" : "+v"(xb[0][0]), "+v"(xb[0][1]), "+v"(xb[1][0]), "+v"(xb[1][1]), "+v"(hb), "+v"(vy));
    __builtin_amdgcn_s_setprio(H_PRIO);
    const char* wsrc = (const char*)P.wo + nt * 128 + (size_t)((hw >> 1) * 80 + (hw & 1) * 20) * 8 * 2048;
    const unsigned wdst = (unsigned)(size_t)smem + ((hw >> 1) * 80 + (hw & 1) * 20) * 1024;
    const int gq = 8 * idx + (lane >> 3);
    unsigned qo = (((gq & 63) + 512 * (gq >> 6) + 256 * (hw >> 1)) * 1024 + 128 * xl + 64 * (hw & 1) + 8 * (lane & 7)) * 2;
    unsigned fb = (unsigned)(size_t)smem + L_FLG + hw * 4;
    asm volatile("" : "+v"(qo), "+v"(fb));
    spin<1>([&]() {
      const u32x2 a = *(volatile LDSAS u32x2*)(smem + F_HR), b = *(volatile LDSAS u32x2*)(smem + F_WH);
      const u32x4 c = *(volatile LDSAS u32x4*)(smem + GATE);
      return __builtin_amdgcn_readfirstlane(a[0] & a[1] & b[0] & b[1] & c[0] & c[1] & c[2] & c[3]) != 0;
    });
    if (hw < 2) { waitv<0>(); lset(smem, F_HR + 8 + hw * 4, 1); }
    STAMP(5);
    asm volatile(CONV_ASM
                 : "=&v"(qv[0]), "=&v"(qv[1]), "=&v"(qv[2]), "=&v"(qv[3])
                 : "v"(xb[0][0]), "v"(xb[0][1]), "v"(xb[1][0]), "v"(xb[1][1]), "v"(hb), "v"(vy), "v"(voW), "v"(qo), "v"(fb),
                   "s"(ry), "s"(wsrc), "s"(wdst), "s"(P.q)
                 : "memory", "vcc", "scc", "s40", "s41", "s42", CONV_CLOBBERS);
    STAMP(6);
  }
  {
    const int u = hw & 1, jh = hw >> 1;
    const int cg = lane & 7, rg = lane >> 3;
    const int gq = 8 * idx + rg;
    const int sq = (gq & 63) + 512 * (gq >> 6) + 256 * jh;
    spin<1>([&]() { return lmin4(smem, F_CVD) != 0; });
#pragma unroll
    for (int i = 0; i < 2; ++i)
      dma16s((const char*)P.wo + nt * 128 + (AH(30) * 4 + hw * 2 + i) * 16384, (unsigned)(size_t)smem + (120 + hw * 2 + i) * 1024, voW);
    waitv<2>();
    gsignal4(P.ws, smem, F_NY, MB_Y, lane * 8 + xl, 32, lane);
    waitv<0>();
    lset(smem, F_WL + hw * 4, 1);
    STAMP(7);
    __builtin_amdgcn_s_setprio(A3_PRIO);
    float qf[4][8], yf[4][8];
    asm volatile("" : "+v"(qv[0]), "+v"(qv[1]), "+v"(qv[2]), "+v"(qv[3]));
#pragma unroll
    for (int j = 0; j < 4; ++j)
#pragma unroll
      for (int c = 0; c < 8; ++c) { qf[j][c] = (c & 1) ? bfhi(qv[j][c >> 1]) : bflo(qv[j][c >> 1]); yf[j][c] = 0.f; pin(qf[j][c]); pin(yf[j][c]); }
    const char* yb = P.ws + WS_Y2T + (128 * xl + 32 * hw) * 4096 + idx * 128;
    unsigned vy[4];
#pragma unroll
    for (int i = 0; i < 4; ++i) vy[i] = rg * 4096 + ((cg ^ ((4 * hw + i) & 7)) << 4) + i * (8 * 4096 - 1024);
    const unsigned yl = (unsigned)(size_t)smem + L_YT + hw * 4096;
    unsigned tr = (unsigned)(size_t)smem + L_YT + (64 * u + 8 * cg) * 128 + ((rg ^ cg) << 4) + jh * 8;
    unsigned oo = (sq * 2048 + 128 * xl + 64 * u + 8 * cg) * 2;
    asm volatile("" : "+s"(yb), "+v"(vy[0]), "+v"(vy[1]), "+v"(vy[2]), "+v"(vy[3]), "+v"(tr), "+v"(oo));
    group_wait<2, true>(smem, F_YR, hw == 0, P.ws + WS_CNT + blockIdx.x * MB_STRIDE + MB_Y * 4, 32);
    STAMP(8);
    asm volatile("s_mov_b32 m0, %0\n\ts_nop 0\n\tglobal_load_lds_dwordx4 %2, %1 sc1\n\tglobal_load_lds_dwordx4 %3, %1 offset:1024 sc1\n\t"
                 "global_load_lds_dwordx4 %4, %1 offset:2048 sc1\n\tglobal_load_lds_dwordx4 %5, %1 offset:3072 sc1\n\ts_waitcnt vmcnt(0)"
                 ::"s"(yl), "s"(yb), "v"(vy[0]), "v"(vy[1]), "v"(vy[2]), "v"(vy[3]) : "memory");
    lset(smem, F_YL + hw * 4, 1);
    spin<1>([&]() { return lmin2(smem, F_YL + u * 8) != 0; });
    sfor<0, 4>([&](auto j_) {
      constexpr int j = decltype(j_)::v;
      float* y = yf[j];
      const unsigned a = tr;
      asm volatile("ds_read_u16_d16_hi %0, %8 offset:%9\n\tds_read_u16_d16_hi %1, %8 offset:%9+128\n\tds_read_u16_d16_hi %2, %8 offset:%9+256\n\t"
                   "ds_read_u16_d16_hi %3, %8 offset:%9+384\n\tds_read_u16_d16_hi %4, %8 offset:%9+512\n\tds_read_u16_d16_hi %5, %8 offset:%9+640\n\t"
                   "ds_read_u16_d16_hi %6, %8 offset:%9+768\n\tds_read_u16_d16_hi %7, %8 offset:%9+896"
                   : "+v"(y[0]), "+v"(y[1]), "+v"(y[2]), "+v"(y[3]), "+v"(y[4]), "+v"(y[5]), "+v"(y[6]), "+v"(y[7]) : "v"(a), "i"(2 * j) : "memory");
      if constexpr (j > 0) {
        float* z = yf[j - 1];
        asm volatile("s_waitcnt lgkmcnt(8)" : "+v"(z[0]), "+v"(z[1]), "+v"(z[2]), "+v"(z[3]), "+v"(z[4]), "+v"(z[5]), "+v"(z[6]), "+v"(z[7])::"memory");
        st_out<OUTAUX3>(u32x4{pack2(qf[j - 1][0] * z[0], qf[j - 1][1] * z[1]), pack2(qf[j - 1][2] * z[2], qf[j - 1][3] * z[3]),
                              pack2(qf[j - 1][4] * z[4], qf[j - 1][5] * z[5]), pack2(qf[j - 1][6] * z[6], qf[j - 1][7] * z[7])}, P.out, oo + (j - 1) * 64 * 4096);
        sched0();
      }
    });
    {
      float* z = yf[3];
      asm volatile("s_waitcnt lgkmcnt(0)" : "+v"(z[0]), "+v"(z[1]), "+v"(z[2]), "+v"(z[3]), "+v"(z[4]), "+v"(z[5]), "+v"(z[6]), "+v"(z[7])::"memory");
      st_out<OUTAUX3>(u32x4{pack2(qf[3][0] * z[0], qf[3][1] * z[1]), pack2(qf[3][2] * z[2], qf[3][3] * z[3]),
                            pack2(qf[3][4] * z[4], qf[3][5] * z[5]), pack2(qf[3][6] * z[6], qf[3][7] * z[7])}, P.out, oo + 3 * 64 * 4096);
    }
  }
  STAMP(9);
  ST_OUT;
  PAD_END;
}

__global__ __launch_bounds__(512, 2) void fused_kernel(Params P) {
  extern __shared__ __attribute__((aligned(16))) char smem_[];
  lptr smem = (lptr)smem_;
  const void* pc0;
  asm volatile("s_getpc_b64 %0" : "=s"(pc0));
  const int lane = threadIdx.x & 63, wid = __builtin_amdgcn_readfirstlane(threadIdx.x >> 6);
  const int xl = blockIdx.x & 7, idx = blockIdx.x >> 3;
  if (__builtin_expect(wid >= 4, 1)) helper_wave(P, smem, wid - 4, lane, xl, idx);
  else compute_wave(P, smem, wid, lane, xl, idx, pc0);
}

#if defined(__HIP_DEVICE_COMPILE__)
#pragma clang attribute pop
#endif

extern "C" void kernel_launch(void* const* d_in, const int* in_sizes, int n_in,
                              void* d_out, int out_size, void* d_ws, size_t ws_size,
                              hipStream_t stream) {
  Params P;
  P.q = (const u16*)d_in[0];
  P.k = (const u16*)d_in[1];
  P.v = (const u16*)d_in[2];
  P.shift_k = (const u16*)d_in[3];
  P.ssm_k = (const u16*)d_in[4];
  P.ssm_D = (const u16*)d_in[5];
  P.x = (const u16*)d_in[6];
  P.wqkv = (const u16*)d_in[7];
  P.wo = (const u16*)d_in[8];
  P.ws = (char*)d_ws;
  P.out = (u16*)d_out;
  (void)hipFuncSetAttribute((const void*)fused_kernel, hipFuncAttributeMaxDynamicSharedMemorySize, SMEM_BYTES);
  fused_kernel<<<256, 512, SMEM_BYTES, stream>>>(P);
}
```
